```python
import torch

def _softmax(x, axis=-1):
    return torch.softmax(x, dim=axis)

def _gelu(x):
    return torch.nn.functional.gelu(x, approximate='tanh')

def _silu(x):
    return torch.nn.functional.silu(x)

def _sigmoid(x):
    return torch.sigmoid(x)

def _layernorm(x, axis=-1, eps=1e-05):
    m = x.mean(dim=axis, keepdim=True)
    v = x.var(dim=axis, keepdim=True, unbiased=False)
    return (x - m) / torch.sqrt(v + eps)

def _scatter_add(tgt, idx, src, axis=0):
    return tgt.clone().index_add_(axis, idx, src)

def _segment_sum(src, seg, n):
    out = torch.zeros((n,) + src.shape[1:], dtype=src.dtype)
    return out.index_add_(0, seg, src)

def _pad(x, pw):
    flat = tuple((v for pair in reversed(pw) for v in pair))
    return torch.nn.functional.pad(x, flat)
kernel_name = 'gated_mix__sliding_local_dilated__mixer_gmlp__prompt'

def setup_inputs():

    _w = lambda a: (lambda t: t.to(torch.bfloat16) if t.is_floating_point() else t)(torch.as_tensor(a))

    def _A_setup():
        B, S, D = (1, 2048, 512)
        return {'x': _w((lambda *s: torch.randn(*s, dtype=torch.bfloat16) * 0.05)(B, S, D)), 'wqkv': _w((lambda *s: torch.randn(*s, dtype=torch.bfloat16) * 0.8)(D, 3 * D)), 'wo': _w((lambda *s: torch.randn(*s, dtype=torch.bfloat16) * 0.05)(D, D))}
    _A_in = _A_setup()

    def _B_setup():
        B, S, D, F = (1, 2048, 512, 1376)
        Fh = F // 2
        return {'x': _w((lambda *s: torch.randn(*s, dtype=torch.bfloat16) * 0.05)(B, S, D)), 'w_in': _w((lambda *s: torch.randn(*s, dtype=torch.bfloat16) * 0.4)(D, 2 * Fh)), 'w_sp': _w(0.01 * (lambda *s: torch.randn(*s, dtype=torch.bfloat16) * 0.4)(S, S)), 'b_sp': _w(1.0 + 0.01 * (lambda *s: torch.randn(*s, dtype=torch.bfloat16) * 0.1)(S)), 'w_out': _w((lambda *s: torch.randn(*s, dtype=torch.bfloat16) * 0.1)(Fh, D))}
    _B_in = _B_setup()
    return {'A_x': _A_in['x'], 'A_wqkv': _A_in['wqkv'], 'A_wo': _A_in['wo'], 'B_x': _B_in['x'], 'B_w_in': _B_in['w_in'], 'B_w_sp': _B_in['w_sp'], 'B_b_sp': _B_in['b_sp'], 'B_w_out': _B_in['w_out'], 'gate_w': _w((lambda *s: torch.randn(*s, dtype=torch.bfloat16) * 0.05)(512) * 16.0)}

def reference(A_x, A_wqkv, A_wo, B_x, B_w_in, B_w_sp, B_b_sp, B_w_out, gate_w):

    def _A_ref(x, wqkv, wo):
        B, S, D = x.shape
        H = 32
        Dh = D // H
        W, dil = (8, 2)
        qkv = (lambda x, *d: x.permute(*d))((x @ wqkv).reshape(B, S, 3, H, Dh), 2, 0, 3, 1, 4)
        q, k, v = (qkv[0], qkv[1], qkv[2])
        scale = Dh ** (-0.5)
        pos = torch.arange(S)
        off = torch.arange(W) * dil
        idx = pos[:, None] - off[None, :]
        mask = idx >= 0
        idx_c = torch.where(mask, idx, 0).reshape(-1)
        kg = k[:, :, idx_c, :].reshape(B, H, S, W, Dh)
        vg = v[:, :, idx_c, :].reshape(B, H, S, W, Dh)
        logits = torch.einsum('bhsd,bhswd->bhsw', q, kg) * scale
        logits = torch.where(mask[None, None, :, :], logits, -1000000000.0)
        attn = _softmax(logits, axis=-1)
        o = torch.einsum('bhsw,bhswd->bhsd', attn, vg)
        return (lambda x, *d: x.permute(*d))(o, 0, 2, 1, 3).reshape(B, S, D) @ wo

    def _B_ref(x, w_in, w_sp, b_sp, w_out):
        B, S, D = x.shape
        F = w_in.shape[-1]
        Fh = F // 2
        z = _gelu(torch.einsum('bsd,df->bsf', x, w_in))
        z1 = z[..., :Fh].clone()
        z2 = _layernorm(z[..., Fh:], axis=-1)
        g = torch.einsum('ts,bsf->btf', w_sp, z2) + b_sp[None, :, None]
        return torch.einsum('bsf,fd->bsd', z1 * g, w_out)
    x = A_x
    g = _sigmoid((x * gate_w).sum(-1, keepdims=True))
    a = _A_ref(x, A_wqkv, A_wo)
    b = _B_ref(x, B_w_in, B_w_sp, B_b_sp, B_w_out)
    return g * a + (1 - g) * b
```

```cpp
#include <hip/hip_runtime.h>
#include <hip/hip_bf16.h>
#include <stdint.h>

typedef short bf16x8 __attribute__((ext_vector_type(8)));
typedef short bf16x4 __attribute__((ext_vector_type(4)));
typedef float f32x4 __attribute__((ext_vector_type(4)));
typedef float f32x2 __attribute__((ext_vector_type(2)));
typedef unsigned u32x4 __attribute__((ext_vector_type(4)));
typedef unsigned u32x2 __attribute__((ext_vector_type(2)));
#define LAS __attribute__((address_space(3)))
typedef LAS unsigned lds_u32;
typedef LAS char lds_char;
typedef __amdgpu_buffer_rsrc_t rsrc_t;

constexpr int S_ = 2048, D_ = 512, F_ = 1376, FH = 688, NQKV = 1536;
constexpr int NFT = 43;
constexpr int NUK = 22;

constexpr size_t WS_OF = 0;
constexpr size_t WS_Z1 = WS_OF + (size_t)128 * 16 * 1024;
constexpr size_t WS_Z2 = WS_Z1 + (size_t)128 * NFT * 512;
constexpr size_t WS_UF = WS_Z2 + (size_t)NFT * 64 * 1024;
constexpr size_t WS_ST = WS_UF + (size_t)128 * NUK * 1024;
constexpr size_t WS_GT = WS_ST + (size_t)2048 * 4 * 8;
constexpr size_t WS_END = WS_GT + 2048 * 4;

#define WAIT_V(n) asm volatile("s_waitcnt vmcnt(%0)" ::"n"(n) : "memory")
#define WAIT_L0() asm volatile("s_waitcnt lgkmcnt(0)" ::: "memory")
#define FENCE() asm volatile("" ::: "memory")
#define BAR() do { asm volatile("" ::: "memory"); __builtin_amdgcn_s_barrier(); asm volatile("" ::: "memory"); } while (0)

__device__ __forceinline__ rsrc_t mk_rsrc(const void* p, unsigned bytes) {
  return __builtin_amdgcn_make_buffer_rsrc((void*)p, 0, bytes, 0x00020000);
}
__device__ __forceinline__ void glds16(rsrc_t r, lds_char* dst, unsigned voff, unsigned soff) {
  __builtin_amdgcn_raw_ptr_buffer_load_lds(r, (lds_u32*)dst, 16, voff, soff, 0, 0);
}
__device__ __forceinline__ unsigned pk_bf16(float lo, float hi) {
  typedef __bf16 bf2 __attribute__((ext_vector_type(2)));
  f32x2 v = {lo, hi};
  return __builtin_bit_cast(unsigned, __builtin_convertvector(v, bf2));
}
__device__ __forceinline__ float bf_lo(unsigned w) { return __uint_as_float(w << 16); }
__device__ __forceinline__ float bf_hi(unsigned w) { return __uint_as_float(w & 0xffff0000u); }
__device__ __forceinline__ bf16x8 lds_rd128(lds_char* p) { return *(LAS bf16x8*)p; }
__device__ __forceinline__ bf16x4 lds_rdtr(lds_char* p) { return __builtin_amdgcn_ds_read_tr16_b64_v4i16((LAS bf16x4*)p); }
template <int OFF>
__device__ __forceinline__ void tr_frag(bf16x4& lo, bf16x4& hi, unsigned addr) {
  asm volatile("ds_read_b64_tr_b16 %0, %2 offset:%3\n\tds_read_b64_tr_b16 %1, %2 offset:%4" : "=&v"(lo), "=&v"(hi) : "v"(addr), "n"(OFF), "n"(OFF + 256) : "memory");
}
#define TR_PIN(x) asm volatile("" : "+v"(x))
__device__ __forceinline__ bf16x8 cat8(bf16x4 a, bf16x4 b) { return __builtin_shufflevector(a, b, 0, 1, 2, 3, 4, 5, 6, 7); }
__device__ __forceinline__ f32x4 mfma32(bf16x8 a, bf16x8 b, f32x4 c) { return __builtin_amdgcn_mfma_f32_16x16x32_bf16(a, b, c, 0, 0, 0); }
__device__ __forceinline__ f32x4 mfma16(bf16x4 a, bf16x4 b, f32x4 c) { return __builtin_amdgcn_mfma_f32_16x16x16bf16_1k(a, b, c, 0, 0, 0); }

__device__ __forceinline__ float rows_max(float v) {
  auto a = __builtin_amdgcn_permlane16_swap(__float_as_uint(v), __float_as_uint(v), false, false);
  v = fmaxf(__uint_as_float(a[0]), __uint_as_float(a[1]));
  auto b = __builtin_amdgcn_permlane32_swap(__float_as_uint(v), __float_as_uint(v), false, false);
  return fmaxf(__uint_as_float(b[0]), __uint_as_float(b[1]));
}
__device__ __forceinline__ float rows_sum(float v) {
  auto a = __builtin_amdgcn_permlane16_swap(__float_as_uint(v), __float_as_uint(v), false, false);
  v = __uint_as_float(a[0]) + __uint_as_float(a[1]);
  auto b = __builtin_amdgcn_permlane32_swap(__float_as_uint(v), __float_as_uint(v), false, false);
  return __uint_as_float(b[0]) + __uint_as_float(b[1]);
}
__device__ __forceinline__ float row16_sum(float v) {
  v += __int_as_float(__builtin_amdgcn_update_dpp(0, __float_as_int(v), 0xB1, 0xf, 0xf, false));
  v += __int_as_float(__builtin_amdgcn_update_dpp(0, __float_as_int(v), 0x4E, 0xf, 0xf, false));
  v += __int_as_float(__builtin_amdgcn_update_dpp(0, __float_as_int(v), 0x141, 0xf, 0xf, false));
  v += __int_as_float(__builtin_amdgcn_update_dpp(0, __float_as_int(v), 0x140, 0xf, 0xf, false));
  return v;
}
__device__ __forceinline__ float gelu_tanh(float x) {
  const float c1 = -2.0f * 0.7978845608028654f * 1.4426950408889634f;
  const float c3 = c1 * 0.044715f;
  float x2 = x * x;
  float t = x * (c1 + c3 * x2);
  float e = __builtin_amdgcn_exp2f(t);
  return x * __builtin_amdgcn_rcpf(1.0f + e);
}
__device__ __forceinline__ u32x4 swap_pair(u32x2 a, u32x2 b) {
  auto x = __builtin_amdgcn_permlane16_swap(a.x, b.x, false, false);
  auto y = __builtin_amdgcn_permlane16_swap(a.y, b.y, false, false);
  u32x4 r; r.x = x[0]; r.y = y[0]; r.z = x[1]; r.w = y[1];
  return r;
}

constexpr int K1_UA = 144 * 64;
constexpr int K1_UB = 32 * 192 * 2;
constexpr int K1_U = K1_UA + K1_UB;
constexpr int K1_NB = 7;
constexpr int K1_LDS = K1_NB * K1_U;

template <int TYPE>
__device__ __forceinline__ void k1_body(lds_char* smem, const __hip_bfloat16* x, const __hip_bfloat16* wqkv,
                                        const __hip_bfloat16* win, char* ws, int panel, int ct, int wid, int lane) {
  constexpr bool ATT = (TYPE == 0);
  const int pitchW = ATT ? NQKV * 2 : F_ * 2;
  rsrc_t rx = mk_rsrc(x, S_ * D_ * 2);
  rsrc_t rw = ATT ? mk_rsrc(wqkv, D_ * NQKV * 2) : mk_rsrc(win, D_ * F_ * 2);
  const int row0 = panel * 128 - (ATT ? 16 : 0);
  int zj = ATT ? 0 : (ct & 3);
  auto a_off = [&](int p) -> unsigned {
    int r = 16 * p + (lane >> 2);
    int gr = row0 + r; gr = gr < 0 ? 0 : gr;
    int ch = (lane & 3) ^ ((-(r >> 2)) & 3);
    return (unsigned)(gr * 1024 + ch * 16);
  };
  auto b_off = [&](int j) -> unsigned {
    int kb = j / 3, i = j - kb * 3;
    int colb = ATT ? (i * 512 + ct * 64) : ((TYPE == 2 ? FH : 0) + zj * 176 + i * 64);
    int col = colb + (lane >> 5) * 32 + (((lane & 3) ^ (2 * (kb & 1))) * 8);
    int k = kb * 8 + ((lane & 31) >> 2);
    return (unsigned)(k * pitchW + col * 2);
  };
  const unsigned off0 = a_off(wid);
  const unsigned off1 = b_off(wid);
  const bool has2 = (wid < 4) || (ATT && wid == 4);
  const unsigned off2 = (wid < 4) ? b_off(8 + wid) : a_off(8);
  auto issue = [&](int u) {
    lds_char* base = smem + (u % K1_NB) * K1_U;
    glds16(rx, base + wid * 1024, off0, u * 64);
    glds16(rw, base + K1_UA + wid * 1024, off1, u * 32 * pitchW);
    if (wid < 4) glds16(rw, base + K1_UA + (8 + wid) * 1024, off2, u * 32 * pitchW);
    else if (ATT && wid == 4) glds16(rx, base + 8 * 1024, off2, u * 64);
    FENCE();
  };
  const int fr = lane & 15, fq = lane >> 4;
  const unsigned a_rd = fr * 64 + ((fq ^ ((-(fr >> 2)) & 3)) * 16);
  const int g = fq, q4 = (lane & 15) >> 2, p4 = lane & 3;
  const unsigned b_rd_e = K1_UA + g * 3072 + q4 * 64 + (2 * (g & 1) + (p4 >> 1)) * 16 + (p4 & 1) * 8;
  const unsigned b_rd_o = K1_UA + g * 3072 + q4 * 64 + (2 * (1 - (g & 1)) + (p4 >> 1)) * 16 + (p4 & 1) * 8;

  constexpr int NACC = ATT ? 14 : 12;
  f32x4 acc[NACC];
#pragma unroll
  for (int i = 0; i < NACC; ++i) acc[i] = (f32x4){0.f, 0.f, 0.f, 0.f};

  const int hw = wid & 3, half = wid >> 2;
  const unsigned lds_base = (unsigned)(size_t)smem;
  const unsigned brd_att = ((hw & 1) ? b_rd_o : b_rd_e) + (hw >> 1) * 512;
  const int znt = half == 0 ? 6 : (zj == 3 ? 4 : 5);

#pragma unroll
  for (int u = 0; u < K1_NB; ++u) issue(u);

#pragma unroll
  for (int u = 0; u < 16; ++u) {
    const int younger = (u == 0) ? 6 : (u <= 10 ? 5 : 15 - u);
    if (has2) { switch (younger) { case 6: WAIT_V(18); break; case 5: WAIT_V(15); break; case 4: WAIT_V(12); break; case 3: WAIT_V(9); break; case 2: WAIT_V(6); break; case 1: WAIT_V(3); break; default: WAIT_V(0); } }
    else      { switch (younger) { case 6: WAIT_V(12); break; case 5: WAIT_V(10); break; case 4: WAIT_V(8); break; case 3: WAIT_V(6); break; case 2: WAIT_V(4); break; case 1: WAIT_V(2); break; default: WAIT_V(0); } }
    BAR();
    if (u >= 1 && u + 6 < 16) issue(u + 6);
    lds_char* base = smem + (u % K1_NB) * K1_U;
    if constexpr (ATT) {
      bf16x8 af[5], bfr[3];
      bf16x4 bl[3], bh[3];
      const unsigned ba = lds_base + (u % K1_NB) * K1_U + brd_att;
      tr_frag<0>(bl[0], bh[0], ba); tr_frag<1024>(bl[1], bh[1], ba); tr_frag<2048>(bl[2], bh[2], ba);
#pragma unroll
      for (int m = 0; m < 5; ++m) af[m] = lds_rd128(base + a_rd + (half * 4 + m) * 1024);
      WAIT_L0();
#pragma unroll
      for (int i = 0; i < 3; ++i) { TR_PIN(bl[i]); TR_PIN(bh[i]); bfr[i] = cat8(bl[i], bh[i]); }
#pragma unroll
      for (int m = 0; m < 4; ++m) acc[m] = mfma32(bfr[0], af[m + 1], acc[m]);
#pragma unroll
      for (int m = 0; m < 5; ++m) acc[4 + m] = mfma32(bfr[1], af[m], acc[4 + m]);
#pragma unroll
      for (int m = 0; m < 5; ++m) acc[9 + m] = mfma32(af[m], bfr[2], acc[9 + m]);
    } else {
      bf16x8 af[2];
      bf16x4 bl[6], bh[6];
      const unsigned be = lds_base + (u % K1_NB) * K1_U + b_rd_e + half * 1536, bo = lds_base + (u % K1_NB) * K1_U + b_rd_o + half * 1536;
      tr_frag<0>(bl[0], bh[0], be); tr_frag<0>(bl[1], bh[1], bo);
      tr_frag<512>(bl[2], bh[2], be); tr_frag<512>(bl[3], bh[3], bo);
      tr_frag<1024>(bl[4], bh[4], be); tr_frag<1024>(bl[5], bh[5], bo);
#pragma unroll
      for (int m = 0; m < 2; ++m) af[m] = lds_rd128(base + a_rd + (hw * 2 + m) * 1024);
      WAIT_L0();
#pragma unroll
      for (int n = 0; n < 6; ++n) {
        TR_PIN(bl[n]); TR_PIN(bh[n]);
        bf16x8 bv = cat8(bl[n], bh[n]);
#pragma unroll
        for (int m = 0; m < 2; ++m) {
          if constexpr (TYPE == 1) acc[m * 6 + n] = mfma32(bv, af[m], acc[m * 6 + n]);
          else acc[m * 6 + n] = mfma32(af[m], bv, acc[m * 6 + n]);
        }
      }
    }
  }

  if constexpr (ATT) {
    bf16x4 qb[4], kb[5], vb[5];
    auto pk4 = [&](f32x4 v) { u32x2 r; r.x = pk_bf16(v[0], v[1]); r.y = pk_bf16(v[2], v[3]); return __builtin_bit_cast(bf16x4, r); };
#pragma unroll
    for (int i = 0; i < 4; ++i) qb[i] = pk4(acc[i]);
#pragma unroll
    for (int i = 0; i < 5; ++i) { kb[i] = pk4(acc[4 + i]); vb[i] = pk4(acc[9 + i]); }
    const int c = fr, par = c & 1;
    const int j0 = 4 * fq + par, j1 = j0 + 2;
    const bool v00 = j0 >= c + 2, v01 = j1 >= c + 2;
    const bool v10 = j0 <= c, v11 = j1 <= c;
    const float sc = 0.25f * 1.4426950408889634f;
    const float NEG = -3.0e38f;
    u32x2 ov[4];
#pragma unroll
    for (int i = 0; i < 4; ++i) {
      f32x4 z4 = {0.f, 0.f, 0.f, 0.f};
      f32x4 s0 = mfma16(kb[i], qb[i], z4);
      f32x4 s1 = mfma16(kb[i + 1], qb[i], z4);
      float a0 = par ? s0[1] : s0[0], a1 = par ? s0[3] : s0[2];
      float b0 = par ? s1[1] : s1[0], b1 = par ? s1[3] : s1[2];
      const bool prev_ok = !(i == 0 && half == 0 && panel == 0);
      const bool m00 = v00 && prev_ok, m01 = v01 && prev_ok;
      a0 = m00 ? a0 : NEG; a1 = m01 ? a1 : NEG; b0 = v10 ? b0 : NEG; b1 = v11 ? b1 : NEG;
      float mx = rows_max(fmaxf(fmaxf(a0, a1), fmaxf(b0, b1)));
      float e00 = m00 ? __builtin_amdgcn_exp2f((a0 - mx) * sc) : 0.f;
      float e01 = m01 ? __builtin_amdgcn_exp2f((a1 - mx) * sc) : 0.f;
      float e10 = v10 ? __builtin_amdgcn_exp2f((b0 - mx) * sc) : 0.f;
      float e11 = v11 ? __builtin_amdgcn_exp2f((b1 - mx) * sc) : 0.f;
      float inv = __builtin_amdgcn_rcpf(rows_sum((e00 + e01) + (e10 + e11)));
      u32x4 pw;
      const unsigned sh = par * 16;
      pw.x = (pk_bf16(e00 * inv, 0.f) & 0xffffu) << sh;
      pw.y = (pk_bf16(e01 * inv, 0.f) & 0xffffu) << sh;
      pw.z = (pk_bf16(e10 * inv, 0.f) & 0xffffu) << sh;
      pw.w = (pk_bf16(e11 * inv, 0.f) & 0xffffu) << sh;
      f32x4 o = mfma32(cat8(vb[i], vb[i + 1]), __builtin_bit_cast(bf16x8, pw), z4);
      ov[i].x = pk_bf16(o[0], o[1]); ov[i].y = pk_bf16(o[2], o[3]);
    }
    const int h = ct * 4 + hw;
    char* of = ws + WS_OF;
#pragma unroll
    for (int i = 0; i < 4; i += 2) {
      u32x4 w = swap_pair(ov[i], ov[i + 1]);
      int t16 = panel * 8 + half * 4 + i + (fq & 1);
      int q = 2 * (h & 1) + (fq >> 1);
      *(u32x4*)(of + ((size_t)(t16 * 16 + (h >> 1)) * 1024) + (q * 16 + c) * 16) = w;
    }
  } else {
    u32x2 zp[12];
#pragma unroll
    for (int n = 0; n < 6; ++n)
#pragma unroll
      for (int m = 0; m < 2; ++m) {
        f32x4 v = acc[m * 6 + n];
        zp[m * 6 + n].x = pk_bf16(gelu_tanh(v[0]), gelu_tanh(v[1]));
        zp[m * 6 + n].y = pk_bf16(gelu_tanh(v[2]), gelu_tanh(v[3]));
      }
    const int ft0 = zj * 11 + half * 6;
    if constexpr (TYPE == 1) {
      char* z1 = ws + WS_Z1;
#pragma unroll
      for (int n = 0; n < 6; ++n)
        if (n < znt) {
#pragma unroll
          for (int m = 0; m < 2; ++m) {
            int t16 = panel * 8 + hw * 2 + m;
            *(u32x2*)(z1 + ((size_t)(t16 * NFT + ft0 + n) * 512) + lane * 8) = zp[m * 6 + n];
          }
        }
    } else {
      char* z2 = ws + WS_Z2;
      const int st = panel * 4 + hw;
      const int q = 2 * (fq & 1) + (fq >> 1);
#pragma unroll
      for (int n = 0; n < 6; ++n)
        if (n < znt) {
          u32x4 w = swap_pair(zp[n], zp[6 + n]);
          *(u32x4*)(z2 + ((size_t)((ft0 + n) * 64 + st) * 1024) + (q * 16 + fr) * 16) = w;
        }
      float mean[8], m2[8];
      const float cnt = (float)(znt * 16), icnt = 1.0f / cnt;
#pragma unroll
      for (int m = 0; m < 2; ++m)
#pragma unroll
        for (int r = 0; r < 4; ++r) {
          float s = 0.f;
#pragma unroll
          for (int n = 0; n < 6; ++n)
            if (n < znt) { unsigned w = (r & 2) ? zp[m * 6 + n].y : zp[m * 6 + n].x; s += (r & 1) ? bf_hi(w) : bf_lo(w); }
          mean[m * 4 + r] = row16_sum(s) * icnt;
        }
#pragma unroll
      for (int m = 0; m < 2; ++m)
#pragma unroll
        for (int r = 0; r < 4; ++r) {
          float s = 0.f;
#pragma unroll
          for (int n = 0; n < 6; ++n)
            if (n < znt) { unsigned w = (r & 2) ? zp[m * 6 + n].y : zp[m * 6 + n].x; float d = ((r & 1) ? bf_hi(w) : bf_lo(w)) - mean[m * 4 + r]; s += d * d; }
          m2[m * 4 + r] = row16_sum(s);
        }
      BAR();
      LAS f32x2* tab = (LAS f32x2*)smem;
      if (half == 1 && fr == 0) {
#pragma unroll
        for (int i = 0; i < 8; ++i) tab[hw * 32 + (i >> 2) * 16 + 4 * fq + (i & 3)] = (f32x2){mean[i], m2[i]};
      }
      WAIT_L0(); BAR();
      if (half == 0 && fr == 0) {
        const float nb = (float)((zj == 3 ? 4 : 5) * 16), na = 96.f, nt = na + nb;
        f32x2* stp = (f32x2*)(ws + WS_ST);
#pragma unroll
        for (int i = 0; i < 8; ++i) {
          int r = hw * 32 + (i >> 2) * 16 + 4 * fq + (i & 3);
          f32x2 o = tab[r];
          float d = o.x - mean[i];
          float mt = mean[i] + d * (nb / nt);
          float q2 = m2[i] + o.y + d * d * (na * nb / nt);
          stp[(size_t)(panel * 128 + r) * 4 + zj] = (f32x2){mt, q2};
        }
      }
    }
  }
}

__global__ __launch_bounds__(512, 2) void gm_k1(const __hip_bfloat16* x, const __hip_bfloat16* wqkv, const __hip_bfloat16* win,
                                                const __hip_bfloat16* gw, char* ws) {
  extern __shared__ __attribute__((aligned(16))) char smem_[];
  lds_char* smem = (lds_char*)smem_;
  const int tid = threadIdx.x, wid = __builtin_amdgcn_readfirstlane(tid >> 6), lane = tid & 63;
  const int xcd = blockIdx.x & 7, idx = blockIdx.x >> 3;
  const int panel = (xcd & 1) * 8 + (idx & 7), ct = (xcd >> 1) * 4 + (idx >> 3);
  const int grow = panel * 128 + ct * 8 + wid;
  u32x4 gx = *(const u32x4*)((const char*)x + (size_t)grow * 1024 + lane * 16);
  u32x4 gv = *(const u32x4*)((const char*)gw + lane * 16);
  if (ct < 8) k1_body<0>(smem, x, wqkv, win, ws, panel, ct, wid, lane);
  else if (ct < 12) k1_body<1>(smem, x, wqkv, win, ws, panel, ct, wid, lane);
  else k1_body<2>(smem, x, wqkv, win, ws, panel, ct, wid, lane);
  float s = 0.f;
#pragma unroll
  for (int i = 0; i < 4; ++i) s += bf_lo(gx[i]) * bf_lo(gv[i]) + bf_hi(gx[i]) * bf_hi(gv[i]);
  s = rows_sum(row16_sum(s));
  if (lane == 0) ((float*)(ws + WS_GT))[grow] = __builtin_amdgcn_rcpf(1.0f + __builtin_amdgcn_exp2f(-s * 1.4426950408889634f));
}

constexpr int K2_NS = 8;
constexpr int K2_TAB = K2_NS * 16384;
constexpr int K2_LDS = K2_TAB + 16384;
constexpr int K2_D = 4;

__global__ __launch_bounds__(512, 2) void gm_k2(const __hip_bfloat16* wsp, const __hip_bfloat16* bsp, char* ws) {
  extern __shared__ __attribute__((aligned(16))) char smem_[];
  lds_char* smem = (lds_char*)smem_;
  const int tid = threadIdx.x, wid = __builtin_amdgcn_readfirstlane(tid >> 6), lane = tid & 63;
  const int xcd = blockIdx.x & 7, idx = blockIdx.x >> 3;
  const int tt = xcd * 4 + (idx & 3), fj = idx >> 2;
  const int ftb = fj < 3 ? fj * 6 : 18 + (fj - 3) * 5;
  const int fh = wid & 1, kg = wid >> 1;
  const int nn = (fh == 0 || fj < 3) ? 3 : 2;
  const int ft0 = ftb + fh * 3;
  const int fr = lane & 15, fq = lane >> 4;
  rsrc_t ra = mk_rsrc(wsp, S_ * S_ * 2);
  rsrc_t rb = mk_rsrc(ws + WS_Z2, NFT * 64 * 1024);

  f32x4 stp[4][2];
  {
    const f32x4* p = (const f32x4*)(ws + WS_ST);
#pragma unroll
    for (int i = 0; i < 4; ++i) { int row = i * 512 + tid; stp[i][0] = p[row * 2]; stp[i][1] = p[row * 2 + 1]; }
  }
  const unsigned aoff = (unsigned)((tt * 64 + wid * 8 + (lane >> 3)) * 4096 + (((lane & 7) ^ (lane >> 3)) * 16));
  auto issueA = [&](int it) {
    lds_char* base = smem + (it % K2_NS) * 16384 + wid * 1024;
    glds16(ra, base, aoff, it * 256);
    glds16(ra, base + 8192, aoff, it * 256 + 128);
  };
  const unsigned boff = (unsigned)((ft0 * 64 + kg) * 1024 + lane * 16);
  u32x4 bq[K2_D][3];
  auto issueB = [&](int it) {
#pragma unroll
    for (int n = 0; n < 3; ++n)
      bq[it % K2_D][n] = __builtin_amdgcn_raw_buffer_load_b128(rb, boff, (it * 4 + (n < 2 ? n : nn - 1) * 64) * 1024, 0);
  };
  const int t16 = tt * 4 + kg;
  u32x2 z1v[3];
#pragma unroll
  for (int n = 0; n < 3; ++n) z1v[n] = *(const u32x2*)(ws + WS_Z1 + ((size_t)(t16 * NFT + ft0 + (n < 2 ? n : nn - 1)) * 512) + lane * 8);
  const float bias = __bfloat162float(bsp[t16 * 16 + fr]);
  FENCE();
#pragma unroll
  for (int it = 0; it < K2_D; ++it) { issueB(it); FENCE(); issueA(it); FENCE(); }
#pragma unroll
  for (int it = K2_D; it < K2_NS; ++it) { issueA(it); FENCE(); }

  {
    LAS float* R = (LAS float*)(smem + K2_TAB);
    LAS float* M = R + 2048;
#pragma unroll
    for (int i = 0; i < 4; ++i) {
      const float m0 = stp[i][0][0], m1 = stp[i][0][2], m2 = stp[i][1][0], m3 = stp[i][1][2];
      const float mean = (176.f * (m0 + m1 + m2) + 160.f * m3) * (1.0f / 688.f);
      const float d0 = m0 - mean, d1 = m1 - mean, d2 = m2 - mean, d3 = m3 - mean;
      const float q = (stp[i][0][1] + stp[i][0][3]) + (stp[i][1][1] + stp[i][1][3]) + 176.f * (d0 * d0 + d1 * d1 + d2 * d2) + 160.f * d3 * d3;
      const float r = 1.0f / sqrtf(q * (1.0f / 688.f) + 1e-5f);
      R[i * 512 + tid] = r; M[i * 512 + tid] = -mean * r;
    }
    WAIT_L0();
  }

  f32x4 acc[4][3];
#pragma unroll
  for (int m = 0; m < 4; ++m)
#pragma unroll
    for (int n = 0; n < 3; ++n) acc[m][n] = (f32x4){0.f, 0.f, 0.f, 0.f};
  const unsigned a_rd = (kg >> 1) * 8192 + fr * 128 + (((4 * (kg & 1) + fq) ^ (fr & 7)) * 16);
  const unsigned t_rd = K2_TAB + (kg * 32 + fq * 8) * 4;

#pragma unroll
  for (int it = 0; it < 16; ++it) {
    constexpr int yw[16] = {23, 21, 21, 21, 24, 27, 30, 33, 33, 33, 31, 29, 27, 22, 17, 12};
    asm volatile("s_waitcnt vmcnt(%0)" ::"n"(yw[it]) : "memory");
    BAR();
    if (it >= 1 && it + 7 < 16) { issueA(it + 7); FENCE(); }
    lds_char* base = smem + (it % K2_NS) * 16384;
    bf16x8 af[4];
#pragma unroll
    for (int m = 0; m < 4; ++m) af[m] = lds_rd128(base + a_rd + m * 2048);
    f32x4 r0 = *(LAS f32x4*)(smem + t_rd + it * 512), r1 = *(LAS f32x4*)(smem + t_rd + it * 512 + 16);
    f32x4 c0 = *(LAS f32x4*)(smem + t_rd + it * 512 + 8192), c1 = *(LAS f32x4*)(smem + t_rd + it * 512 + 8192 + 16);
#pragma unroll
    for (int n = 0; n < 3; ++n) {
      {
        u32x4 z = bq[it % K2_D][n];
        u32x4 y;
        y.x = pk_bf16(bf_lo(z.x) * r0[0] + c0[0], bf_hi(z.x) * r0[1] + c0[1]);
        y.y = pk_bf16(bf_lo(z.y) * r0[2] + c0[2], bf_hi(z.y) * r0[3] + c0[3]);
        y.z = pk_bf16(bf_lo(z.z) * r1[0] + c1[0], bf_hi(z.z) * r1[1] + c1[1]);
        y.w = pk_bf16(bf_lo(z.w) * r1[2] + c1[2], bf_hi(z.w) * r1[3] + c1[3]);
        bf16x8 zb = __builtin_bit_cast(bf16x8, y);
#pragma unroll
        for (int m = 0; m < 4; ++m) acc[m][n] = mfma32(zb, af[m], acc[m][n]);
      }
    }
    WAIT_L0();
    if (it + K2_D < 16) issueB(it + K2_D);
  }
  BAR();
#pragma unroll
  for (int m = 0; m < 4; ++m)
    if (m != kg) {
#pragma unroll
      for (int n = 0; n < 3; ++n) *(LAS f32x4*)(smem + (((fh * 4 + m) * 4 + kg) * 3 + n) * 1024 + lane * 16) = acc[m][n];
    }
  WAIT_L0(); BAR();
  f32x4 gsum[3];
#pragma unroll
  for (int n = 0; n < 3; ++n) {
    gsum[n] = kg == 0 ? acc[0][n] : (kg == 1 ? acc[1][n] : (kg == 2 ? acc[2][n] : acc[3][n]));
  }
#pragma unroll
  for (int s = 0; s < 4; ++s)
    if (s != kg) {
#pragma unroll
      for (int n = 0; n < 3; ++n) gsum[n] += *(LAS f32x4*)(smem + (((fh * 4 + kg) * 4 + s) * 3 + n) * 1024 + lane * 16);
    }
  char* uf = ws + WS_UF;
#pragma unroll
  for (int n = 0; n < 3; ++n)
    if (n < nn) {
      const int ft = ft0 + n;
      u32x2 w;
      w.x = pk_bf16(bf_lo(z1v[n].x) * (gsum[n][0] + bias), bf_hi(z1v[n].x) * (gsum[n][1] + bias));
      w.y = pk_bf16(bf_lo(z1v[n].y) * (gsum[n][2] + bias), bf_hi(z1v[n].y) * (gsum[n][3] + bias));
      char* fb = uf + (size_t)(t16 * NUK + (ft >> 1)) * 1024;
      *(u32x2*)(fb + ((2 * (ft & 1) + (fq >> 1)) * 16 + fr) * 16 + (fq & 1) * 8) = w;
      if (ft == NFT - 1) *(u32x2*)(fb + 512 + lane * 8) = (u32x2){0u, 0u};
    }
}

constexpr int K3_LDS = 19 * 8192;

__global__ __launch_bounds__(512, 2) void gm_k3(const __hip_bfloat16* wo, const __hip_bfloat16* wout, const char* ws, __hip_bfloat16* out) {
  extern __shared__ __attribute__((aligned(16))) char smem_[];
  lds_char* smem = (lds_char*)smem_;
  const int tid = threadIdx.x, wid = __builtin_amdgcn_readfirstlane(tid >> 6), lane = tid & 63;
  const int xcd = blockIdx.x & 7, idx = blockIdx.x >> 3;
  const int tt = xcd * 4 + (idx & 3), nj = idx >> 2;
  const int mw = wid & 3, kp = wid >> 2;
  const int fr = lane & 15, fq = lane >> 4;
  const int t16 = tt * 4 + mw;
  rsrc_t rwo = mk_rsrc(wo, D_ * D_ * 2);
  rsrc_t rwt = mk_rsrc(wout, FH * D_ * 2);
  rsrc_t rof = mk_rsrc(ws + WS_OF, 128 * 16 * 1024);
  rsrc_t ruf = mk_rsrc(ws + WS_UF, 128 * NUK * 1024);
  if (wid >= 6) *(LAS u32x4*)(smem + 18 * 8192 + wid * 1024 + lane * 16) = (u32x4){0u, 0u, 0u, 0u};
  const unsigned boff = (unsigned)((wid * 8 + ((lane & 31) >> 2)) * 1024 + (nj * 64 + (lane >> 5) * 32 + (((lane & 3) ^ (2 * (wid & 1))) * 8)) * 2);
  u32x4 aq[19];
  const float gate = ((const float*)(ws + WS_GT))[t16 * 16 + fr];
  FENCE();
#pragma unroll
  for (int kt = 0; kt < 19; ++kt) {
    if (kt < 8) {
      aq[kt] = __builtin_amdgcn_raw_buffer_load_b128(rof, lane * 16, (t16 * 16 + kt * 2 + kp) * 1024, 0);
      glds16(rwo, smem + kt * 8192 + wid * 1024, boff, kt * 65536);
    } else {
      aq[kt] = __builtin_amdgcn_raw_buffer_load_b128(ruf, lane * 16, (t16 * NUK + (kt - 8) * 2 + kp) * 1024, 0);
      if (kt < 18 || wid < 6) glds16(rwt, smem + kt * 8192 + wid * 1024, boff, (kt - 8) * 65536);
      else glds16(rwt, smem + kt * 8192 + (wid - 2) * 1024, boff - 2 * 8192, (kt - 8) * 65536);
    }
    FENCE();
  }
  WAIT_L0();
  const int g = fq, q4 = (lane & 15) >> 2, p4 = lane & 3;
  const unsigned b_rd_e = (4 * kp + g) * 1024 + q4 * 64 + (2 * (g & 1) + (p4 >> 1)) * 16 + (p4 & 1) * 8;
  const unsigned b_rd_o = (4 * kp + g) * 1024 + q4 * 64 + (2 * (1 - (g & 1)) + (p4 >> 1)) * 16 + (p4 & 1) * 8;
  const unsigned lds_base = (unsigned)(size_t)smem;
  f32x4 acca[4], accb[4];
#pragma unroll
  for (int n = 0; n < 4; ++n) { acca[n] = (f32x4){0.f, 0.f, 0.f, 0.f}; accb[n] = (f32x4){0.f, 0.f, 0.f, 0.f}; }
#pragma unroll
  for (int kt = 0; kt < 19; ++kt) {
    asm volatile("s_waitcnt vmcnt(%0)" ::"n"(2 * (18 - kt)) : "memory");
    BAR();
    bf16x8 av = __builtin_bit_cast(bf16x8, aq[kt]);
    bf16x4 bl[4], bh[4];
    const unsigned be = lds_base + kt * 8192 + b_rd_e, bo = lds_base + kt * 8192 + b_rd_o;
    tr_frag<0>(bl[0], bh[0], be); tr_frag<0>(bl[1], bh[1], bo); tr_frag<512>(bl[2], bh[2], be); tr_frag<512>(bl[3], bh[3], bo);
    WAIT_L0();
#pragma unroll
    for (int n = 0; n < 4; ++n) {
      TR_PIN(bl[n]); TR_PIN(bh[n]);
      bf16x8 bv = cat8(bl[n], bh[n]);
      if (kt < 8) acca[n] = mfma32(bv, av, acca[n]);
      else accb[n] = mfma32(bv, av, accb[n]);
    }
  }
  WAIT_L0(); BAR();
#pragma unroll
  for (int n = 0; n < 4; ++n) {
    f32x4 y = acca[n] * gate + accb[n] * (1.0f - gate);
    *(LAS f32x4*)(smem + kp * 17408 + (mw * 16 + fr) * 272 + (n * 16 + fq * 4) * 4) = y;
  }
  WAIT_L0(); BAR();
  {
    const int r = tid >> 3, ch = tid & 7;
    lds_char* p = smem + r * 272 + ch * 32;
    f32x4 a0 = *(LAS f32x4*)p + *(LAS f32x4*)(p + 17408);
    f32x4 a1 = *(LAS f32x4*)(p + 16) + *(LAS f32x4*)(p + 17408 + 16);
    u32x4 w;
    w.x = pk_bf16(a0[0], a0[1]); w.y = pk_bf16(a0[2], a0[3]); w.z = pk_bf16(a1[0], a1[1]); w.w = pk_bf16(a1[2], a1[3]);
    *(u32x4*)((char*)out + (size_t)(tt * 64 + r) * 1024 + nj * 128 + ch * 16) = w;
  }
}

extern "C" void kernel_launch(void* const* d_in, const int* in_sizes, int n_in, void* d_out, int out_size, void* d_ws,
                              size_t ws_size, hipStream_t stream) {
  const __hip_bfloat16* x = (const __hip_bfloat16*)d_in[0];
  const __hip_bfloat16* wqkv = (const __hip_bfloat16*)d_in[1];
  const __hip_bfloat16* wo = (const __hip_bfloat16*)d_in[2];
  const __hip_bfloat16* win = (const __hip_bfloat16*)d_in[4];
  const __hip_bfloat16* wsp = (const __hip_bfloat16*)d_in[5];
  const __hip_bfloat16* bsp = (const __hip_bfloat16*)d_in[6];
  const __hip_bfloat16* wout = (const __hip_bfloat16*)d_in[7];
  const __hip_bfloat16* gw = (const __hip_bfloat16*)d_in[8];
  char* ws = (char*)d_ws;
  (void)hipFuncSetAttribute((const void*)gm_k1, hipFuncAttributeMaxDynamicSharedMemorySize, K1_LDS);
  (void)hipFuncSetAttribute((const void*)gm_k2, hipFuncAttributeMaxDynamicSharedMemorySize, K2_LDS);
  (void)hipFuncSetAttribute((const void*)gm_k3, hipFuncAttributeMaxDynamicSharedMemorySize, K3_LDS);
  gm_k1<<<256, 512, K1_LDS, stream>>>(x, wqkv, win, gw, ws);
  gm_k2<<<256, 512, K2_LDS, stream>>>(wsp, bsp, ws);
  gm_k3<<<256, 512, K3_LDS, stream>>>(wo, wout, ws, (__hip_bfloat16*)d_out);
}
```

```cpp
#include <hip/hip_runtime.h>
#include <hip/hip_bf16.h>
#include <stdint.h>
#include <type_traits>

typedef short bf16x8 __attribute__((ext_vector_type(8)));
typedef short bf16x4 __attribute__((ext_vector_type(4)));
typedef float f32x4 __attribute__((ext_vector_type(4)));
typedef float f32x2 __attribute__((ext_vector_type(2)));
typedef unsigned u32x4 __attribute__((ext_vector_type(4)));
typedef unsigned u32x2 __attribute__((ext_vector_type(2)));
typedef unsigned long long u64;
#define LAS __attribute__((address_space(3)))
#define GAS __attribute__((address_space(1)))
typedef LAS unsigned lds_u32;
typedef LAS char lds_char;
typedef __amdgpu_buffer_rsrc_t rsrc_t;

constexpr int S_ = 2048, D_ = 512, F_ = 1376, FH = 688, NQKV = 1536;
constexpr int NFT = 43;
constexpr int NUK = 22;

constexpr size_t WS_OF = 0;
constexpr size_t WS_Z1 = WS_OF + (size_t)128 * 16 * 1024;
constexpr size_t WS_Z2 = WS_Z1 + (size_t)64 * NFT * 1024;
constexpr size_t WS_UF = WS_Z2 + (size_t)NFT * 64 * 1024;
constexpr size_t WS_ST = WS_UF + (size_t)128 * NUK * 1024;
constexpr size_t WS_GT = WS_ST + (size_t)2048 * 4 * 16;
constexpr size_t WS_FA = WS_GT + 2048 * 4;
constexpr size_t WS_F2 = WS_FA + 1024;
constexpr size_t WS_F1 = WS_F2 + 8192;
constexpr size_t WS_FU = WS_F1 + 1024;
constexpr size_t WS_EP = WS_FU + 2048;
constexpr size_t WS_END = WS_EP + 256;

constexpr int SLOT = 8192;
constexpr int LDS_BYTES = 20 * SLOT;
constexpr int AUX_SC1 = 16;

#define STAMP(i)
#define WAIT_V(n) asm volatile("s_waitcnt vmcnt(%0)" ::"n"(n) : "memory")
#define WAIT_L(n) asm volatile("s_waitcnt lgkmcnt(%0)" ::"n"(n) : "memory")
#define FENCE() asm volatile("" ::: "memory")
#define BAR() do { asm volatile("" ::: "memory"); __builtin_amdgcn_s_barrier(); asm volatile("" ::: "memory"); } while (0)
#define SCHED() __builtin_amdgcn_sched_barrier(0)
#define PIN(x) asm volatile("" : "+v"(x))
#define RLX_AGENT __ATOMIC_RELAXED, __HIP_MEMORY_SCOPE_AGENT
#define P2IT(it) (((it) & ~3) + (((it) + rot2) & 3))

__device__ __forceinline__ rsrc_t mk_rsrc(const void* p, unsigned bytes) {
  return __builtin_amdgcn_make_buffer_rsrc((void*)p, 0, bytes, 0x00020000);
}
template <int AUX = 0>
__device__ __forceinline__ void glds16(rsrc_t r, lds_char* dst, unsigned voff, unsigned soff) {
  __builtin_amdgcn_raw_ptr_buffer_load_lds(r, (lds_u32*)dst, 16, voff, soff, 0, AUX);
}
__device__ __forceinline__ void st16_sc1(rsrc_t r, unsigned voff, u32x4 v) { __builtin_amdgcn_raw_buffer_store_b128(v, r, voff, 0, AUX_SC1); }
__device__ __forceinline__ u32x4 ld16_sc1(rsrc_t r, unsigned voff, unsigned soff) { return __builtin_amdgcn_raw_buffer_load_b128(r, voff, soff, AUX_SC1); }
__device__ __forceinline__ unsigned pk_bf16(float lo, float hi) {
  typedef __bf16 bf2 __attribute__((ext_vector_type(2)));
  f32x2 v = {lo, hi};
  return __builtin_bit_cast(unsigned, __builtin_convertvector(v, bf2));
}
__device__ __forceinline__ float fma1(float a, float b, float c) { float d; asm("v_fma_f32 %0, %1, %2, %3" : "=v"(d) : "v"(a), "v"(b), "v"(c)); return d; }
__device__ __forceinline__ float bf_lo(unsigned w) { return __uint_as_float(w << 16); }
__device__ __forceinline__ float bf_hi(unsigned w) { return __uint_as_float(w & 0xffff0000u); }
template <int OFF, class T>
__device__ __forceinline__ void rd128(T& d, unsigned addr) {
  asm volatile("ds_read_b128 %0, %1 offset:%2" : "=v"(d) : "v"(addr), "n"(OFF) : "memory");
}
template <int OFF>
__device__ __forceinline__ void tr_frag(bf16x4& lo, bf16x4& hi, unsigned addr) {
  asm volatile("ds_read_b64_tr_b16 %0, %2 offset:%3\n\tds_read_b64_tr_b16 %1, %2 offset:%4" : "=&v"(lo), "=&v"(hi) : "v"(addr), "n"(OFF), "n"(OFF + 256) : "memory");
}
__device__ __forceinline__ bf16x8 cat8(bf16x4 a, bf16x4 b) { return __builtin_shufflevector(a, b, 0, 1, 2, 3, 4, 5, 6, 7); }
__device__ __forceinline__ f32x4 mfma32(bf16x8 a, bf16x8 b, f32x4 c) { return __builtin_amdgcn_mfma_f32_16x16x32_bf16(a, b, c, 0, 0, 0); }
__device__ __forceinline__ void keep2(bf16x8 a, bf16x8 b) { asm volatile("" :: "v"(a), "v"(b)); }
__device__ __forceinline__ f32x4 mfma16(bf16x4 a, bf16x4 b, f32x4 c) { return __builtin_amdgcn_mfma_f32_16x16x16bf16_1k(a, b, c, 0, 0, 0); }

__device__ __forceinline__ float rows_max(float v) {
  auto a = __builtin_amdgcn_permlane16_swap(__float_as_uint(v), __float_as_uint(v), false, false);
  v = fmaxf(__uint_as_float(a[0]), __uint_as_float(a[1]));
  auto b = __builtin_amdgcn_permlane32_swap(__float_as_uint(v), __float_as_uint(v), false, false);
  return fmaxf(__uint_as_float(b[0]), __uint_as_float(b[1]));
}
__device__ __forceinline__ float rows_sum(float v) {
  auto a = __builtin_amdgcn_permlane16_swap(__float_as_uint(v), __float_as_uint(v), false, false);
  v = __uint_as_float(a[0]) + __uint_as_float(a[1]);
  auto b = __builtin_amdgcn_permlane32_swap(__float_as_uint(v), __float_as_uint(v), false, false);
  return __uint_as_float(b[0]) + __uint_as_float(b[1]);
}
__device__ __forceinline__ float row16_sum(float v) {
  v += __int_as_float(__builtin_amdgcn_update_dpp(0, __float_as_int(v), 0xB1, 0xf, 0xf, false));
  v += __int_as_float(__builtin_amdgcn_update_dpp(0, __float_as_int(v), 0x4E, 0xf, 0xf, false));
  v += __int_as_float(__builtin_amdgcn_update_dpp(0, __float_as_int(v), 0x141, 0xf, 0xf, false));
  v += __int_as_float(__builtin_amdgcn_update_dpp(0, __float_as_int(v), 0x140, 0xf, 0xf, false));
  return v;
}
__device__ __forceinline__ float gelu_tanh(float x) {
  const float c1 = -2.0f * 0.7978845608028654f * 1.4426950408889634f;
  const float c3 = c1 * 0.044715f;
  float t = x * (c1 + c3 * (x * x));
  return x * __builtin_amdgcn_rcpf(1.0f + __builtin_amdgcn_exp2f(t));
}
__device__ __forceinline__ f32x4 gelu4(f32x4 v) { return (f32x4){gelu_tanh(v[0]), gelu_tanh(v[1]), gelu_tanh(v[2]), gelu_tanh(v[3])}; }
__device__ __forceinline__ u32x4 swap_pair(u32x2 a, u32x2 b) {
  auto x = __builtin_amdgcn_permlane16_swap(a.x, b.x, false, false);
  auto y = __builtin_amdgcn_permlane16_swap(a.y, b.y, false, false);
  u32x4 r; r.x = x[0]; r.y = y[0]; r.z = x[1]; r.w = y[1];
  return r;
}
__device__ __forceinline__ u32x2 pk4(f32x4 v) { u32x2 r; r.x = pk_bf16(v[0], v[1]); r.y = pk_bf16(v[2], v[3]); return r; }
__device__ __forceinline__ u64 ldflag(const char* ws, unsigned off) { return __hip_atomic_load((GAS u64*)(ws + off), RLX_AGENT); }
__device__ __forceinline__ void poll_flags(const char* ws, unsigned off, u64 tag) {
#pragma unroll 1
  for (unsigned spins = 0; spins < (1u << 20); ++spins) {
    if (__all(ldflag(ws, off) == tag)) break;
    __builtin_amdgcn_s_sleep(1);
  }
}
template <int N> using IC = std::integral_constant<int, N>;
template <int N, class F> __device__ __forceinline__ void sfor(F f) {
  if constexpr (N > 0) { sfor<N - 1>(f); f(IC<N - 1>{}); }
}

constexpr int PA_BUF = 6 * SLOT;
constexpr int PA_BOFF = 18432;
constexpr int PB_BUF = 3 * SLOT;
#define ROTWB 3
#define ROTW1 3

__global__ __launch_bounds__(512, 2) void gm_fused(const __hip_bfloat16* x, const __hip_bfloat16* wqkv, const __hip_bfloat16* wo,
                                                   const __hip_bfloat16* win, const __hip_bfloat16* wsp, const __hip_bfloat16* bsp,
                                                   const __hip_bfloat16* wout, const __hip_bfloat16* gw, char* ws, __hip_bfloat16* out) {
  extern __shared__ __attribute__((aligned(16))) char smem_[];
  STAMP(0);
  lds_char* smem = (lds_char*)smem_;
  const unsigned lds = (unsigned)(size_t)smem;
  const int tid = threadIdx.x;
  const int wid = __builtin_amdgcn_readfirstlane(tid >> 6), lane = tid & 63, fr = lane & 15, fq = lane >> 4;
  const int xcd = blockIdx.x & 7, idx = blockIdx.x >> 3;
  const int tt = xcd * 4 + (idx & 3), fj = idx >> 2, nj = fj;
  const int rot2 = (fj + idx) & 3;
  const int g = fq, q4 = (lane & 15) >> 2, p4 = lane & 3;
  const unsigned tr_e = q4 * 64 + (2 * (g & 1) + (p4 >> 1)) * 16 + (p4 & 1) * 8;
  const unsigned tr_o = q4 * 64 + (2 * (1 - (g & 1)) + (p4 >> 1)) * 16 + (p4 & 1) * 8;
  const unsigned sw8 = ((lane & 7) ^ (lane >> 3)) * 16;
  rsrc_t rx = mk_rsrc(x, S_ * D_ * 2);
  rsrc_t rsp = mk_rsrc(wsp, S_ * S_ * 2);
  const unsigned sp_off = (unsigned)((tt * 64 + wid * 8 + (lane >> 3)) * 4096) + sw8;
  auto pf_sp = [&](int s) { glds16(rsp, smem + s * SLOT + wid * 1024, sp_off, P2IT(s >> 1) * 256 + (s & 1) * 128); };
  u64 tag, fz_a;
  unsigned myflag;
  constexpr int LB = 6;
  const int ftb = fj < 3 ? fj * 6 : 18 + (fj - 3) * 5;
  const int nft = fj < 3 ? 6 : 5;
  const int fh = wid & 1, kg = wid >> 1;
  const int ft0 = ftb + fh * 3;
  const bool has2 = fh * 3 + 2 < nft;
  rsrc_t rz2 = mk_rsrc(ws + WS_Z2, NFT * 64 * 1024);
  const unsigned zb_off = (unsigned)((ft0 * 64 + kg) * 1024 + lane * 16);
  const unsigned zb_n2 = has2 ? 2 * 65536u : 65536u;
  const unsigned f2_off = (unsigned)(WS_F2 + xcd * 1024 + ((lane >> 1) * 4 + ((lane & 1) ? (ftb + nft - 1) / 11 : ftb / 11)) * 8);
  u32x4 bq[LB][3];
  auto issueB = [&](int it) {
    const unsigned so = (unsigned)(P2IT(it) * 4096);
    bq[it % LB][0] = ld16_sc1(rz2, zb_off, so); bq[it % LB][1] = ld16_sc1(rz2, zb_off, so + 65536u); bq[it % LB][2] = ld16_sc1(rz2, zb_off, so + zb_n2);
  };
  auto p2_first = [&]() {
#pragma unroll
    for (int it = 0; it < LB; ++it) issueB(it);
    FENCE();
  };
  bool early;

  if (xcd < 4) {
    const int panel = (xcd & 1) * 8 + (idx & 7), ct = (xcd >> 1) * 4 + (idx >> 3);
    const int rot1 = (idx & 7) + 2 * (idx >> 3);
    rsrc_t rw = mk_rsrc(wqkv, D_ * NQKV * 2);
    const int row0 = panel * 128 - 16;
    auto a_off = [&](int p) -> unsigned { int gr = row0 + 8 * p + (lane >> 3); gr = gr < 0 ? 0 : gr; return (unsigned)(gr * 1024) + sw8; };
    const unsigned v0 = a_off(wid), v1 = a_off(wid + 8), v5 = a_off(wid + 10);
    const unsigned v2 = (unsigned)((wid * 8 + ((lane & 31) >> 2)) * (NQKV * 2) + (ct * 64 + (lane >> 5) * 32 + (((lane & 3) ^ (2 * (wid & 1))) * 8)) * 2);
    lds_char* const dA = smem + wid * 1024; lds_char* const dB = smem + PA_BOFF + wid * 3072;
    auto LD = [&](int i, int u) {
      const int kt = (u & ~ROTW1) + ((u + rot1) & ROTW1);
      const int bo = (u % 3) * PA_BUF;
      if (i == 0) glds16(rx, dA + bo, v0, kt * 128);
      if (i == 1) glds16(rw, dB + bo, v2, kt * (64 * NQKV * 2));
      if (i == 2) glds16(rx, dA + bo + 8192, v1, kt * 128);
      if (i == 3) glds16(rw, dB + bo + 1024, v2, kt * (64 * NQKV * 2) + 1024);
      if (i == 4) glds16(rw, dB + bo + 2048, v2, kt * (64 * NQKV * 2) + 2048);
      if (i == 5 && wid >= 6) glds16(rx, dA + bo + 10240, v5, kt * 128);
    };
#pragma unroll
    for (int u = 0; u < 3; ++u) {
#pragma unroll
      for (int i = 0; i < 6; ++i) LD(i, u);
      if (u == 0) BAR();
      FENCE();
    }
    STAMP(1);
    const int hw = wid & 3, half = wid >> 2;
    const unsigned a_rd0 = lds + (half * 64 + fr) * 128 + ((fq ^ (fr & 7)) * 16), a_rd1 = a_rd0 ^ 64u;
    const unsigned b_rd = lds + PA_BOFF + g * 3072 + ((hw & 1) ? tr_o : tr_e) + (hw >> 1) * 512;
    f32x4 acc[14];
#pragma unroll
    for (int i = 0; i < 14; ++i) acc[i] = (f32x4){0.f, 0.f, 0.f, 0.f};
    struct Set { bf16x8 a[2][5]; bf16x4 l[2][3], h[2][3]; };
    Set X, Y;
    auto rdg = [&](Set& s, auto U, auto J) {
      constexpr int u = decltype(U)::value, j = decltype(J)::value, ks = j >> 3, i = j & 7;
      constexpr int bo = (u % 3 == 1) ? PA_BUF : 0;
      const unsigned aa = (ks ? a_rd1 : a_rd0) + (u % 3 == 2 ? 2u * PA_BUF : 0u), bb = b_rd + (u % 3) * (unsigned)PA_BUF;
      if constexpr (i == 0) tr_frag<ks * 12288>(s.l[ks][0], s.h[ks][0], bb);
      else if constexpr (i <= 4) rd128<bo + i * 2048>(s.a[ks][i], aa);
      else if constexpr (i == 5) tr_frag<ks * 12288 + 1024>(s.l[ks][1], s.h[ks][1], bb);
      else if constexpr (i == 6) rd128<bo>(s.a[ks][0], aa);
      else tr_frag<ks * 12288 + 2048>(s.l[ks][2], s.h[ks][2], bb);
    };
    WAIT_V(10); BAR();
    STAMP(2);
    sfor<16>([&](auto J) { rdg(X, IC<0>{}, J); });
    const int grow = panel * 128 + ct * 16 + wid * 2;
    sfor<8>([&](auto U) {
      constexpr int u = decltype(U)::value;
      Set& c = (u & 1) ? Y : X; Set& n = (u & 1) ? X : Y;
      WAIT_L(0);
      WAIT_V(5); BAR();
#pragma unroll
      for (int ks = 0; ks < 2; ++ks) {
#pragma unroll
        for (int i = 0; i < 5; ++i) PIN(c.a[ks][i]);
#pragma unroll
        for (int i = 0; i < 3; ++i) { PIN(c.l[ks][i]); PIN(c.h[ks][i]); }
      }
      sfor<28>([&](auto J) {
        constexpr int j = decltype(J)::value, ks = j / 14, i = j % 14;
        if constexpr (i < 4) acc[i] = mfma32(cat8(c.l[ks][0], c.h[ks][0]), c.a[ks][i + 1], acc[i]);
        else if constexpr (i < 9) acc[i] = mfma32(cat8(c.l[ks][1], c.h[ks][1]), c.a[ks][i - 4], acc[i]);
        else acc[i] = mfma32(c.a[ks][i - 9], cat8(c.l[ks][2], c.h[ks][2]), acc[i]);
        SCHED();
        if constexpr (j % 4 == 0) {
          if constexpr (u < 5) { if constexpr (j / 4 < 5) LD(j / 4, u + 3); }
          else if constexpr (j / 4 < 6) pf_sp((u % 3) * 6 + j / 4);
        }
        constexpr int r = j - (j + 3) / 4;
        if constexpr (u < 7 && j % 4 != 0 && r < 16) rdg(n, IC<u + 1>{}, IC<r>{});
        SCHED();
      });
      if constexpr (u < 5) LD(5, u + 3);
      STAMP(8 + u);
    });
    WAIT_L(0);
    tag = ldflag(ws, WS_EP);
    fz_a = ldflag(ws, f2_off);
    const u32x4 gx0 = *(const u32x4*)((const char*)x + (size_t)grow * 1024 + lane * 16);
    const u32x4 gx1 = *(const u32x4*)((const char*)x + (size_t)grow * 1024 + 1024 + lane * 16);
    const u32x4 gv = *(const u32x4*)((const char*)gw + lane * 16);
    FENCE();
    STAMP(3);
    bf16x4 qb[4], kb[5], vb[5];
#pragma unroll
    for (int i = 0; i < 4; ++i) qb[i] = __builtin_bit_cast(bf16x4, pk4(acc[i]));
#pragma unroll
    for (int i = 0; i < 5; ++i) { kb[i] = __builtin_bit_cast(bf16x4, pk4(acc[4 + i])); vb[i] = __builtin_bit_cast(bf16x4, pk4(acc[9 + i])); }
    const int par = fr & 1;
    const int j0 = 4 * fq + par, j1 = j0 + 2;
    const bool v00 = j0 >= fr + 2, v01 = j1 >= fr + 2, v10 = j0 <= fr, v11 = j1 <= fr;
    const float sc = 0.25f * 1.4426950408889634f, NEG = -3.0e38f;
    u32x2 ov[4];
#pragma unroll
    for (int i = 0; i < 4; ++i) {
      f32x4 z4 = {0.f, 0.f, 0.f, 0.f};
      f32x4 s0 = mfma16(kb[i], qb[i], z4);
      f32x4 s1 = mfma16(kb[i + 1], qb[i], z4);
      float a0 = par ? s0[1] : s0[0], a1 = par ? s0[3] : s0[2];
      float b0 = par ? s1[1] : s1[0], b1 = par ? s1[3] : s1[2];
      const bool prev_ok = !(i == 0 && half == 0 && panel == 0);
      const bool m00 = v00 && prev_ok, m01 = v01 && prev_ok;
      a0 = m00 ? a0 : NEG; a1 = m01 ? a1 : NEG; b0 = v10 ? b0 : NEG; b1 = v11 ? b1 : NEG;
      float mx = rows_max(fmaxf(fmaxf(a0, a1), fmaxf(b0, b1)));
      float e00 = m00 ? __builtin_amdgcn_exp2f((a0 - mx) * sc) : 0.f;
      float e01 = m01 ? __builtin_amdgcn_exp2f((a1 - mx) * sc) : 0.f;
      float e10 = v10 ? __builtin_amdgcn_exp2f((b0 - mx) * sc) : 0.f;
      float e11 = v11 ? __builtin_amdgcn_exp2f((b1 - mx) * sc) : 0.f;
      float inv = __builtin_amdgcn_rcpf(rows_sum((e00 + e01) + (e10 + e11)));
      u32x4 pw;
      const unsigned sh = par * 16;
      pw.x = (pk_bf16(e00 * inv, 0.f) & 0xffffu) << sh;
      pw.y = (pk_bf16(e01 * inv, 0.f) & 0xffffu) << sh;
      pw.z = (pk_bf16(e10 * inv, 0.f) & 0xffffu) << sh;
      pw.w = (pk_bf16(e11 * inv, 0.f) & 0xffffu) << sh;
      ov[i] = pk4(mfma32(cat8(vb[i], vb[i + 1]), __builtin_bit_cast(bf16x8, pw), z4));
    }
    tag += 1ull;
    early = __all(fz_a == tag);
    if (early) p2_first();
    const int h = ct * 4 + hw;
    rsrc_t ro = mk_rsrc(ws + WS_OF, 128 * 16 * 1024);
#pragma unroll
    for (int i = 0; i < 4; i += 2) {
      const int t16 = panel * 8 + half * 4 + i + (fq & 1);
      const int q = 2 * (h & 1) + (fq >> 1);
      st16_sc1(ro, (unsigned)((t16 * 16 + (h >> 1)) * 1024 + (q * 16 + fr) * 16), swap_pair(ov[i], ov[i + 1]));
    }
    float s0 = 0.f, s1 = 0.f;
#pragma unroll
    for (int i = 0; i < 4; ++i) {
      s0 += bf_lo(gx0[i]) * bf_lo(gv[i]) + bf_hi(gx0[i]) * bf_hi(gv[i]);
      s1 += bf_lo(gx1[i]) * bf_lo(gv[i]) + bf_hi(gx1[i]) * bf_hi(gv[i]);
    }
    s0 = rows_sum(row16_sum(s0)); s1 = rows_sum(row16_sum(s1));
    const float gt = __builtin_amdgcn_rcpf(1.0f + __builtin_amdgcn_exp2f(-(lane == 0 ? s0 : s1) * 1.4426950408889634f));
    if (lane < 2) __hip_atomic_store((GAS unsigned*)(ws + WS_GT) + grow + lane, __float_as_uint(gt), RLX_AGENT);
    myflag = (unsigned)(WS_FA + (panel * 8 + ct) * 8);
  } else {
    const int r64 = (xcd & 1) * 16 + (idx & 15), zj = ((xcd >> 1) & 1) * 2 + (idx >> 4);
    constexpr int pitchW = F_ * 2;
    rsrc_t rw = mk_rsrc(win, D_ * pitchW);
    const unsigned ox = (unsigned)((r64 * 64 + wid * 8 + (lane >> 3)) * 1024) + sw8;
    auto b_off = [&](int i) -> unsigned {
      int lc = i * 64 + (lane >> 5) * 32 + (((lane & 3) ^ (2 * (wid & 1))) * 8);
      if (lc >= (zj == 3 ? 160 : 176)) lc -= (zj == 3 ? 32 : 16);
      return (unsigned)((wid * 8 + ((lane & 31) >> 2)) * pitchW + (zj * 176 + lc) * 2);
    };
    const unsigned ob0 = b_off(0), ob1 = b_off(1), ob2 = b_off(2);
    auto issueW = [&](int i, int v) {
      const int kt = (v & 7 & ~ROTWB) + ((v + idx) & ROTWB);
      const unsigned so = (unsigned)(kt * (64 * pitchW) + (v < 8 ? FH * 2 : 0));
      lds_char* d = smem + 8 * SLOT + (v & 3) * PB_BUF + wid * 3072;
      if (i == 0 && v < 8) glds16(rx, smem + v * SLOT + wid * 1024, ox, kt * 128);
      if (i == 1) glds16(rw, d, ob0, so);
      if (i == 2) glds16(rw, d + 1024, ob1, so);
      if (i == 3) glds16(rw, d + 2048, ob2, so);
    };
#pragma unroll
    for (int v = 0; v < 4; ++v) {
#pragma unroll
      for (int i = 0; i < 4; ++i) issueW(i, v);
      if (v == 0) { BAR(); tag = ldflag(ws, WS_EP); }
      FENCE();
    }
    STAMP(1);
    const int mp = wid & 1, nq = wid >> 1;
    const unsigned a_rd0 = lds + (mp * 32 + fr) * 128 + ((fq ^ (fr & 7)) * 16), a_rd1 = a_rd0 ^ 64u;
    unsigned b_rd[3];
#pragma unroll
    for (int n = 0; n < 3; ++n) { const int t = nq * 3 + n; b_rd[n] = lds + 8 * SLOT + g * 3072 + ((t & 1) ? tr_o : tr_e) + (t >> 1) * 512; }
    f32x4 acc[2][3], g2[2][3];
#pragma unroll
    for (int i = 0; i < 6; ++i) acc[i / 3][i % 3] = (f32x4){0.f, 0.f, 0.f, 0.f};
    struct Set { bf16x8 a[2][2]; bf16x4 l[2][3], h[2][3]; };
    Set X, Y;
    auto rdg = [&](Set& s, auto V, auto J) {
      constexpr int v = decltype(V)::value, j = decltype(J)::value, ks = j / 5, i = j % 5;
      constexpr int xo = (v & 7) * SLOT, wo_ = (v & 3) * PB_BUF;
      if constexpr (i == 0) rd128<xo>(s.a[ks][0], ks ? a_rd1 : a_rd0);
      else if constexpr (i == 2) rd128<xo + 2048>(s.a[ks][1], ks ? a_rd1 : a_rd0);
      else {
        constexpr int n = i == 1 ? 0 : i - 2;
        if constexpr (wo_ < 2 * PB_BUF) tr_frag<wo_ + ks * 12288>(s.l[ks][n], s.h[ks][n], b_rd[n]);
        else tr_frag<wo_ - 2 * PB_BUF + ks * 12288>(s.l[ks][n], s.h[ks][n], b_rd[n] + 2 * PB_BUF);
      }
    };
    const int nval = (zj == 3 ? 10 : 11) - nq * 3;
    rsrc_t rst = mk_rsrc(ws + WS_ST, 2048 * 64);
    u32x4 pt0, pt1;
    const unsigned goff = (unsigned)((r64 * 64 + mp * 32 + (lane >> 1)) * 64 + (lane & 1) * 32);
    WAIT_V(13); BAR();
    STAMP(2);
    sfor<10>([&](auto J) { rdg(X, IC<0>{}, J); });
    sfor<16>([&](auto V) {
      constexpr int v = decltype(V)::value;
      Set& c = (v & 1) ? Y : X; Set& n = (v & 1) ? X : Y;
      constexpr int wv[16] = {8, 8, 8, 8, 8, 7, 6, 6, 3, 6, 6, 6, 6, 6, 10, 10};
      WAIT_L(0);
      WAIT_V(wv[v]); BAR();
#pragma unroll
      for (int ks = 0; ks < 2; ++ks) {
        PIN(c.a[ks][0]); PIN(c.a[ks][1]);
#pragma unroll
        for (int i = 0; i < 3; ++i) { PIN(c.l[ks][i]); PIN(c.h[ks][i]); }
      }
      if constexpr (v == 13) {
        if (tid < 8) __hip_atomic_store((GAS u64*)(ws + WS_F2) + tid * 128 + r64 * 4 + zj, tag, RLX_AGENT);
      }
      SCHED();
      sfor<12>([&](auto J) {
        constexpr int j = decltype(J)::value, ks = j / 6, nn = (j % 6) >> 1, m = j & 1;
        if constexpr (v >= 8) acc[m][nn] = mfma32(cat8(c.l[ks][nn], c.h[ks][nn]), c.a[ks][m], acc[m][nn]);
        else acc[m][nn] = mfma32(c.a[ks][m], cat8(c.l[ks][nn], c.h[ks][nn]), acc[m][nn]);
        SCHED();
        if constexpr (j % 2 == 0) {
          constexpr int k = j / 2;
          if constexpr (v < 7) { if constexpr (k < 4) issueW(k, v + 4); }
          else if constexpr (v == 8) issueW(1 + k % 3, 11 + k / 3);
          else if constexpr (v >= 9 && v <= 11) { if constexpr (k < 3) issueW(1 + k, v + 4); }
          else if constexpr (v == 12) pf_sp(k < 5 ? k : 8);
          else if constexpr (v == 13) { if constexpr (k < 5) pf_sp(k == 0 ? 5 : 8 + k); }
          else if constexpr (v == 14) { if constexpr (k < 5) pf_sp(k == 0 ? 6 : 12 + k); }
          else if constexpr (v == 15) { if constexpr (k < 2) pf_sp(k == 0 ? 7 : 17); }
        }
        if constexpr (v < 15 && j < 10) rdg(n, IC<v + 1>{}, J);
        SCHED();
      });
      if constexpr (v == 7) {
        STAMP(4);
        WAIT_L(0);
        PIN(tag); tag += 1ull;
        const float w1 = nval > 1 ? 1.f : 0.f, w2 = nval > 2 ? 1.f : 0.f;
        const float icnt = nval > 2 ? (1.f / 48.f) : (nval > 1 ? (1.f / 32.f) : (1.f / 16.f));
        LAS f32x2* tab = (LAS f32x2*)(smem + 8 * SLOT + 3 * PB_BUF);
#pragma unroll
        for (int m = 0; m < 2; ++m) {
#pragma unroll
          for (int nn = 0; nn < 3; ++nn) { g2[m][nn] = gelu4(acc[m][nn]); acc[m][nn] = (f32x4){0.f, 0.f, 0.f, 0.f}; }
          const f32x4 z4 = {0.f, 0.f, 0.f, 0.f};
          const f32x4 a1 = nval > 1 ? g2[m][1] : z4, a2 = nval > 2 ? g2[m][2] : z4;
          f32x4 sm = g2[m][0] + a1 + a2, mean, m2;
#pragma unroll
          for (int r = 0; r < 4; ++r) mean[r] = row16_sum(sm[r]) * icnt;
          const f32x4 d0 = g2[m][0] - mean, d1 = (a1 - mean) * w1, d2 = (a2 - mean) * w2;
          sm = d0 * d0 + d1 * d1 + d2 * d2;
#pragma unroll
          for (int r = 0; r < 4; ++r) m2[r] = row16_sum(sm[r]);
          if (fr < 4) {
            const float mv = fr == 0 ? mean[0] : (fr == 1 ? mean[1] : (fr == 2 ? mean[2] : mean[3]));
            const float qv = fr == 0 ? m2[0] : (fr == 1 ? m2[1] : (fr == 2 ? m2[2] : m2[3]));
            tab[(mp * 32 + m * 16 + 4 * fq + fr) * 4 + nq] = (f32x2){mv, qv};
          }
        }
        WAIT_L(0); BAR();
        if (wid == 0) {
          const f32x4 p01 = *(LAS f32x4*)(tab + lane * 4), p23 = *(LAS f32x4*)(tab + lane * 4 + 2);
          const float n3 = zj == 3 ? 16.f : 32.f, nt = 144.f + n3;
          const float mu = (48.f * (p01[0] + p01[2] + p23[0]) + n3 * p23[2]) / nt;
          const float d0 = p01[0] - mu, d1 = p01[2] - mu, d2 = p23[0] - mu, d3 = p23[2] - mu;
          const float q = (p01[1] + p01[3]) + (p23[1] + p23[3]) + 48.f * (d0 * d0 + d1 * d1 + d2 * d2) + n3 * d3 * d3;
          st16_sc1(rst, (unsigned)((r64 * 64 + lane) * 64 + zj * 16), (u32x4){__float_as_uint(mu), (unsigned)tag, __float_as_uint(q), (unsigned)(tag >> 32)});
        }
        STAMP(5);
      }
      if constexpr (v == 10) { pt0 = ld16_sc1(rst, goff, 0); pt1 = ld16_sc1(rst, goff, 16); FENCE(); }
      if constexpr (v == 11) {
        WAIT_L(0);
        const unsigned tlo = (unsigned)tag, thi = (unsigned)(tag >> 32);
        if (!__all(pt0.y == tlo && pt0.w == thi && pt1.y == tlo && pt1.w == thi)) {
#pragma unroll 1
          for (unsigned spins = 0; spins < (1u << 20); ++spins) {
            pt0 = ld16_sc1(rst, goff, 0); pt1 = ld16_sc1(rst, goff, 16);
            if (__all(pt0.y == tlo && pt0.w == thi && pt1.y == tlo && pt1.w == thi)) break;
            __builtin_amdgcn_s_sleep(2);
          }
        }
        const float a0 = __uint_as_float(pt0.x), a1 = __uint_as_float(pt1.x), n1 = (lane & 1) ? 160.f : 176.f;
        float sm = 176.f * a0 + n1 * a1;
        sm += __int_as_float(__builtin_amdgcn_update_dpp(0, __float_as_int(sm), 0xB1, 0xf, 0xf, false));
        const float mu = sm * (1.0f / 688.f), d0 = a0 - mu, d1 = a1 - mu;
        float q = __uint_as_float(pt0.z) + __uint_as_float(pt1.z) + 176.f * d0 * d0 + n1 * d1 * d1;
        q += __int_as_float(__builtin_amdgcn_update_dpp(0, __float_as_int(q), 0xB1, 0xf, 0xf, false));
        const float rstd = 1.0f / sqrtf(q * (1.0f / 688.f) + 1e-5f);
        LAS float* tb = (LAS float*)(smem + wid * 1024);
        tb[(lane & 1) * 32 + (lane >> 1)] = (lane & 1) ? -mu * rstd : rstd;
        WAIT_L(0);
        const f32x4 r0 = *(LAS f32x4*)(tb + 4 * fq), r1 = *(LAS f32x4*)(tb + 16 + 4 * fq);
        const f32x4 c0 = *(LAS f32x4*)(tb + 32 + 4 * fq), c1 = *(LAS f32x4*)(tb + 48 + 4 * fq);
        const int q2 = 2 * (fq & 1) + (fq >> 1);
#pragma unroll
        for (int nn = 0; nn < 3; ++nn)
          if (nn < nval)
            st16_sc1(rz2, (unsigned)(((zj * 11 + nq * 3 + nn) * 64 + r64 * 2 + mp) * 1024 + (q2 * 16 + fr) * 16),
                     swap_pair(pk4(g2[0][nn] * r0 + c0), pk4(g2[1][nn] * r1 + c1)));
        FENCE();
      }
      if constexpr (v == 14) { fz_a = ldflag(ws, f2_off); FENCE(); }
    });
    STAMP(3);
    early = __all(fz_a == tag);
    if (early) p2_first();
    {
      rsrc_t rz1 = mk_rsrc(ws + WS_Z1, 64 * NFT * 1024);
#pragma unroll
      for (int n = 0; n < 3; ++n)
        if (n < nval) {
          const u32x2 a = pk4(gelu4(acc[0][n])), b = pk4(gelu4(acc[1][n]));
          st16_sc1(rz1, (unsigned)(((r64 * 2 + mp) * NFT + zj * 11 + nq * 3 + n) * 1024 + lane * 16), (u32x4){a.x, a.y, b.x, b.y});
        }
    }
    myflag = (unsigned)(WS_F1 + (r64 * 4 + zj) * 8);
  }
  STAMP(6);
  WAIT_V(0); BAR();
  if (tid == 0) __hip_atomic_store((GAS u64*)(ws + myflag), tag, RLX_AGENT);
  STAMP(7);
  if (wid == 0 && !early) poll_flags(ws, f2_off, tag);
  BAR();
  STAMP(8);
  if (!early) p2_first();

  rsrc_t rwt = mk_rsrc(wout, FH * D_ * 2);
  const unsigned wt_off = (unsigned)((wid * 8 + ((lane & 31) >> 2)) * 1024 + (nj * 64 + (lane >> 5) * 32 + (((lane & 3) ^ (2 * (wid & 1))) * 8)) * 2);
  const int t16 = tt * 4 + kg;
  const int mw = wid & 3, kp = wid >> 2, t16a = tt * 4 + mw;
  f32x4 acc[4][3];
#pragma unroll
  for (int i = 0; i < 12; ++i) acc[i / 3][i % 3] = (f32x4){0.f, 0.f, 0.f, 0.f};
  const unsigned a_rd = lds + (kg >> 1) * SLOT + fr * 128 + (((4 * (kg & 1) + fq) ^ (fr & 7)) * 16);
  bf16x8 XA[4], YA[4];
  auto rd2 = [&](bf16x8 (&a)[4], auto IT, auto M) {
    constexpr int it = decltype(IT)::value, m = decltype(M)::value, off = (it % 9) * 2 * SLOT + m * 2048;
    if constexpr (off < 65536) rd128<off>(a[m], a_rd); else if constexpr (off < 131072) rd128<off - 65536>(a[m], a_rd + 65536u); else rd128<off - 131072>(a[m], a_rd + 131072u);
  };
  u64 f1v = 0, fav = 0; u32x2 z1v[3]; float bias = 0.f;
  STAMP(9);
  sfor<4>([&](auto M) { rd2(XA, IC<0>{}, M); });
  sfor<16>([&](auto IT) {
    constexpr int it = decltype(IT)::value;
    bf16x8 (&c)[4] = (it & 1) ? YA : XA; bf16x8 (&n)[4] = (it & 1) ? XA : YA;
    WAIT_L(0);
    if constexpr (it == 8) WAIT_V(9);
    if constexpr (it == 12) WAIT_V(3);
    if constexpr (it == 4 || it == 8 || it == 12 || it == 14) BAR();
    SCHED();
#pragma unroll
    for (int i = 0; i < 4; ++i) PIN(c[i]);
    sfor<12>([&](auto J) {
      constexpr int j = decltype(J)::value, nn = j >> 2, m = j & 3;
      acc[m][nn] = mfma32(__builtin_bit_cast(bf16x8, bq[it % LB][nn]), c[m], acc[m][nn]);
      SCHED();
      if constexpr (it < 15 && j % 2 == 0 && j < 8) rd2(n, IC<it + 1>{}, IC<j / 2>{});
      if constexpr ((it == 4 || it == 5 || it == 6 || it == 8) && (j == 1 || j == 3 || j == 5 || j == 7)) {
        constexpr int sl = (it == 8 ? 10 : (it - 4) * 4) + (j >> 1);
        if constexpr (!(it == 6 && j > 3))
          glds16(rsp, smem + sl * SLOT + wid * 1024, sp_off, P2IT((sl >> 1) + 9) * 256 + (sl & 1) * 128);
      }
      if constexpr (it >= 12 && it < 14 && (j == 1 || j == 3 || j == 5 || j == 7)) {
        constexpr int kt = (it - 12) * 4 + (j >> 1);
        glds16(rwt, smem + kt * SLOT + wid * 1024, wt_off, kt * 65536);
      }
      if constexpr (it == 14 && (j == 1 || j == 3 || j == 5)) {
        constexpr int kt = 8 + (j >> 1);
        glds16(rwt, smem + kt * SLOT + wid * 1024, wt_off - ((kt == 10 && wid >= 6) ? 16384u : 0u), kt * 65536);
      }
      if constexpr (j % 4 == 3 && it + LB < 16)
        bq[it % LB][nn] = ld16_sc1(rz2, zb_off, (unsigned)(P2IT(it + LB) * 4096) + (nn == 0 ? 0u : (nn == 1 ? 65536u : zb_n2)));
      SCHED();
    });
    if constexpr (it == 8) f1v = ldflag(ws, (unsigned)(WS_F1 + (tt * 4 + (lane & 3)) * 8));
    if constexpr (it == 12) {
      if (!__all(f1v == tag)) poll_flags(ws, (unsigned)(WS_F1 + (tt * 4 + (lane & 3)) * 8), tag);
      rsrc_t rz1 = mk_rsrc(ws + WS_Z1, 64 * NFT * 1024);
#pragma unroll
      for (int nn = 0; nn < 3; ++nn)
        z1v[nn] = __builtin_amdgcn_raw_buffer_load_b64(rz1, (unsigned)(((t16 >> 1) * NFT + ft0 + (nn < 2 || has2 ? nn : 0)) * 1024 + lane * 16 + (t16 & 1) * 8), 0, AUX_SC1);
      bias = __bfloat162float(bsp[t16 * 16 + fr]);
      fav = ldflag(ws, (unsigned)(WS_FA + ((tt >> 1) * 8 + (lane & 7)) * 8));
    }
    SCHED();
  });
  STAMP(10);
  BAR();
  lds_char* red = smem + 11 * SLOT;
  rsrc_t ruf = mk_rsrc(ws + WS_UF, 128 * NUK * 1024);
  {
#pragma unroll
    for (int m = 0; m < 4; ++m)
      if (m != kg) {
        const int src = kg - (kg > m ? 1 : 0);
#pragma unroll
        for (int n = 0; n < 3; ++n) *(LAS f32x4*)(red + (((fh * 4 + m) * 3 + src) * 3 + n) * 1024 + lane * 16) = acc[m][n];
      }
    WAIT_L(0); BAR();
    u32x2 uv[3];
#pragma unroll
    for (int n = 0; n < 3; ++n) {
      f32x4 gs = (kg == 0 ? acc[0][n] : (kg == 1 ? acc[1][n] : (kg == 2 ? acc[2][n] : acc[3][n]))) + bias;
#pragma unroll
      for (int s = 0; s < 3; ++s) gs += *(LAS f32x4*)(red + (((fh * 4 + kg) * 3 + s) * 3 + n) * 1024 + lane * 16);
      uv[n].x = pk_bf16(bf_lo(z1v[n].x) * gs[0], bf_hi(z1v[n].x) * gs[1]);
      uv[n].y = pk_bf16(bf_lo(z1v[n].y) * gs[2], bf_hi(z1v[n].y) * gs[3]);
    }
    if (!has2) uv[2] = (u32x2){0u, 0u};
    {
      const int ft = ft0 + (fq & 1);
      st16_sc1(ruf, (unsigned)((t16 * NUK + (ft >> 1)) * 1024 + ((2 * (ft & 1) + (fq >> 1)) * 16 + fr) * 16), swap_pair(uv[0], uv[1]));
    }
    if (has2 || ft0 + 2 == NFT) {
      const int ft = ft0 + 2;
      const u32x4 w = swap_pair(uv[2], uv[2]);
      if (!(fq & 1)) st16_sc1(ruf, (unsigned)((t16 * NUK + (ft >> 1)) * 1024 + ((2 * (ft & 1) + (fq >> 1)) * 16 + fr) * 16), w);
    }
  }
  WAIT_L(0); BAR();
  STAMP(11);
  const unsigned w_rd_e = lds + (4 * kp + g) * 1024 + tr_e, w_rd_o = lds + (4 * kp + g) * 1024 + tr_o;
  f32x4 acca[4];
#pragma unroll
  for (int n = 0; n < 4; ++n) acca[n] = (f32x4){0.f, 0.f, 0.f, 0.f};
  float gate;
  {
    rsrc_t rwo = mk_rsrc(wo, D_ * D_ * 2);
#pragma unroll
    for (int k = 0; k < 8; ++k) glds16(rwo, smem + (11 + k) * SLOT + wid * 1024, wt_off, k * 65536);
    FENCE();
    WAIT_V(8); BAR();
    if (tid == 0) __hip_atomic_store((GAS u64*)(ws + WS_FU) + tt * 8 + fj, tag, RLX_AGENT);
    STAMP(12);
    if (!__all(fav == tag)) poll_flags(ws, (unsigned)(WS_FA + ((tt >> 1) * 8 + (lane & 7)) * 8), tag);
    rsrc_t rof = mk_rsrc(ws + WS_OF, 128 * 16 * 1024);
    u32x4 aq[8];
#pragma unroll
    for (int i = 0; i < 8; ++i) aq[i] = ld16_sc1(rof, lane * 16, (t16a * 16 + i * 2 + kp) * 1024);
    gate = __uint_as_float(__hip_atomic_load((GAS unsigned*)(ws + WS_GT) + t16a * 16 + fr, RLX_AGENT));
    FENCE();
    if (blockIdx.x == 0 && wid == 7) {
      poll_flags(ws, (unsigned)(WS_FA + lane * 8), tag); poll_flags(ws, (unsigned)(WS_FA + 512 + lane * 8), tag);
      poll_flags(ws, (unsigned)(WS_F1 + lane * 8), tag); poll_flags(ws, (unsigned)(WS_F1 + 512 + lane * 8), tag);
      if (lane == 0) __hip_atomic_store((GAS u64*)(ws + WS_EP), tag, RLX_AGENT);
    }
    WAIT_V(9); BAR();
#pragma unroll
    for (int i = 0; i < 8; ++i) {
      bf16x4 bl[4], bh[4];
      const unsigned so = (11 + i) * SLOT;
      tr_frag<0>(bl[0], bh[0], w_rd_e + so); tr_frag<0>(bl[1], bh[1], w_rd_o + so);
      tr_frag<512>(bl[2], bh[2], w_rd_e + so); tr_frag<512>(bl[3], bh[3], w_rd_o + so);
      WAIT_L(0);
#pragma unroll
      for (int n = 0; n < 4; ++n) { PIN(bl[n]); PIN(bh[n]); acca[n] = mfma32(cat8(bl[n], bh[n]), __builtin_bit_cast(bf16x8, aq[i]), acca[n]); }
    }
  }
  STAMP(13);
  {
    bf16x4 wl[11][4], wh[11][4];
#pragma unroll
    for (int i = 0; i < 11; ++i) {
      const unsigned so = i * SLOT;
      tr_frag<0>(wl[i][0], wh[i][0], w_rd_e + so); tr_frag<0>(wl[i][1], wh[i][1], w_rd_o + so);
      tr_frag<512>(wl[i][2], wh[i][2], w_rd_e + so); tr_frag<512>(wl[i][3], wh[i][3], w_rd_o + so);
      if ((i & 1) || i == 10) WAIT_L(0);
    }
    if (wid == 0) poll_flags(ws, (unsigned)(WS_FU + (tt * 8 + (lane & 7)) * 8), tag);
    BAR();
    STAMP(14);
    u32x4 aq[11];
#pragma unroll
    for (int i = 0; i < 11; ++i) aq[i] = ld16_sc1(ruf, lane * 16, (t16a * NUK + i * 2 + kp) * 1024);
    f32x4 accb[4];
#pragma unroll
    for (int n = 0; n < 4; ++n) accb[n] = (f32x4){0.f, 0.f, 0.f, 0.f};
#pragma unroll
    for (int i = 0; i < 11; ++i) {
#pragma unroll
      for (int n = 0; n < 4; ++n) { PIN(wl[i][n]); PIN(wh[i][n]); accb[n] = mfma32(cat8(wl[i][n], wh[i][n]), __builtin_bit_cast(bf16x8, aq[i]), accb[n]); }
    }
#pragma unroll
    for (int n = 0; n < 4; ++n)
      *(LAS f32x4*)(red + kp * 17408 + (mw * 16 + fr) * 272 + (n * 16 + fq * 4) * 4) = acca[n] * gate + accb[n] * (1.0f - gate);
    WAIT_L(0); BAR();
    const int r = tid >> 3, ch = tid & 7;
    lds_char* p = red + r * 272 + ch * 32;
    f32x4 a0 = *(LAS f32x4*)p + *(LAS f32x4*)(p + 17408);
    f32x4 a1 = *(LAS f32x4*)(p + 16) + *(LAS f32x4*)(p + 17408 + 16);
    u32x4 w;
    w.x = pk_bf16(a0[0], a0[1]); w.y = pk_bf16(a0[2], a0[3]); w.z = pk_bf16(a1[0], a1[1]); w.w = pk_bf16(a1[2], a1[3]);
    *(u32x4*)((char*)out + (size_t)(tt * 64 + r) * 1024 + nj * 128 + ch * 16) = w;
  }
  STAMP(15);
}

extern "C" void kernel_launch(void* const* d_in, const int* in_sizes, int n_in, void* d_out, int out_size, void* d_ws,
                              size_t ws_size, hipStream_t stream) {
  (void)hipFuncSetAttribute((const void*)gm_fused, hipFuncAttributeMaxDynamicSharedMemorySize, LDS_BYTES);
  gm_fused<<<256, 512, LDS_BYTES, stream>>>((const __hip_bfloat16*)d_in[0], (const __hip_bfloat16*)d_in[1], (const __hip_bfloat16*)d_in[2],
                                            (const __hip_bfloat16*)d_in[4], (const __hip_bfloat16*)d_in[5], (const __hip_bfloat16*)d_in[6],
                                            (const __hip_bfloat16*)d_in[7], (const __hip_bfloat16*)d_in[8], (char*)d_ws, (__hip_bfloat16*)d_out);
}
```

```cpp
#define WARM 0
#include <hip/hip_runtime.h>
#include <hip/hip_bf16.h>
#include <stdint.h>
#include <type_traits>

typedef short bf16x8 __attribute__((ext_vector_type(8)));
typedef short bf16x4 __attribute__((ext_vector_type(4)));
typedef float f32x4 __attribute__((ext_vector_type(4)));
typedef float f32x2 __attribute__((ext_vector_type(2)));
typedef unsigned u32x4 __attribute__((ext_vector_type(4)));
typedef unsigned u32x2 __attribute__((ext_vector_type(2)));
typedef unsigned long long u64;
#define LAS __attribute__((address_space(3)))
#define GAS __attribute__((address_space(1)))
typedef LAS unsigned lds_u32;
typedef LAS char lds_char;
typedef __amdgpu_buffer_rsrc_t rsrc_t;

constexpr int S_ = 2048, D_ = 512, F_ = 1376, FH = 688, NQKV = 1536;
constexpr int NFT = 43;
constexpr int NUK = 22;

constexpr size_t MB4 = 4u << 20;
constexpr size_t WS_OF = 0 * MB4 + 4096;
constexpr size_t WS_Z1 = 1 * MB4 + 4096;
constexpr size_t WS_Z2 = 2 * MB4 + 4096;
constexpr size_t WS_UF = 3 * MB4 + 4096;
constexpr size_t WS_ST = 4 * MB4 + 4096;
constexpr size_t WS_GT = WS_ST + (size_t)2048 * 4 * 16;
constexpr size_t WS_FA = WS_GT + 2048 * 4;
constexpr size_t WS_F2 = WS_FA + 1024;
constexpr size_t WS_F1 = WS_F2 + 8192;
constexpr size_t WS_FU = WS_F1 + 1024;
constexpr size_t WS_EP = WS_FU + 2048;
constexpr size_t WS_END = WS_EP + 256;

constexpr int SLOT = 8192;
constexpr int LDS_BYTES = 20 * SLOT;
constexpr int AUX_SC1 = 16;

#define STAMP(i)
#define WAIT_V(n) asm volatile("s_waitcnt vmcnt(%0)" ::"n"(n) : "memory")
#define WAIT_L(n) asm volatile("s_waitcnt lgkmcnt(%0)" ::"n"(n) : "memory")
#define FENCE() asm volatile("" ::: "memory")
#define BAR() do { asm volatile("" ::: "memory"); __builtin_amdgcn_s_barrier(); asm volatile("" ::: "memory"); } while (0)
#define SCHED() __builtin_amdgcn_sched_barrier(0)
#define PIN(x) asm volatile("" : "+v"(x))
#define RLX_AGENT __ATOMIC_RELAXED, __HIP_MEMORY_SCOPE_AGENT
#define P2IT(it) (((it) & ~3) + (((it) + rot2) & 3))

__device__ __forceinline__ rsrc_t mk_rsrc(const void* p, unsigned bytes) {
  return __builtin_amdgcn_make_buffer_rsrc((void*)p, 0, bytes, 0x00020000);
}
template <int AUX = 0>
__device__ __forceinline__ void glds16(rsrc_t r, lds_char* dst, unsigned voff, unsigned soff) {
  __builtin_amdgcn_raw_ptr_buffer_load_lds(r, (lds_u32*)dst, 16, voff, soff, 0, AUX);
}
__device__ __forceinline__ void st16_sc1(rsrc_t r, unsigned voff, u32x4 v) { __builtin_amdgcn_raw_buffer_store_b128(v, r, voff, 0, AUX_SC1); }
__device__ __forceinline__ u32x4 ld16_sc1(rsrc_t r, unsigned voff, unsigned soff) { return __builtin_amdgcn_raw_buffer_load_b128(r, voff, soff, AUX_SC1); }
__device__ __forceinline__ unsigned pk_bf16(float lo, float hi) {
  typedef __bf16 bf2 __attribute__((ext_vector_type(2)));
  f32x2 v = {lo, hi};
  return __builtin_bit_cast(unsigned, __builtin_convertvector(v, bf2));
}
__device__ __forceinline__ float fma1(float a, float b, float c) { float d; asm("v_fma_f32 %0, %1, %2, %3" : "=v"(d) : "v"(a), "v"(b), "v"(c)); return d; }
__device__ __forceinline__ float bf_lo(unsigned w) { return __uint_as_float(w << 16); }
__device__ __forceinline__ float bf_hi(unsigned w) { return __uint_as_float(w & 0xffff0000u); }
template <int OFF, class T>
__device__ __forceinline__ void rd128(T& d, unsigned addr) {
  asm volatile("ds_read_b128 %0, %1 offset:%2" : "=v"(d) : "v"(addr), "n"(OFF) : "memory");
}
template <int OFF>
__device__ __forceinline__ void tr_frag(bf16x4& lo, bf16x4& hi, unsigned addr) {
  asm volatile("ds_read_b64_tr_b16 %0, %2 offset:%3\n\tds_read_b64_tr_b16 %1, %2 offset:%4" : "=&v"(lo), "=&v"(hi) : "v"(addr), "n"(OFF), "n"(OFF + 256) : "memory");
}
__device__ __forceinline__ bf16x8 cat8(bf16x4 a, bf16x4 b) { return __builtin_shufflevector(a, b, 0, 1, 2, 3, 4, 5, 6, 7); }
__device__ __forceinline__ f32x4 mfma32(bf16x8 a, bf16x8 b, f32x4 c) { return __builtin_amdgcn_mfma_f32_16x16x32_bf16(a, b, c, 0, 0, 0); }
__device__ __forceinline__ void keep2(bf16x8 a, bf16x8 b) { asm volatile("" :: "v"(a), "v"(b)); }
__device__ __forceinline__ f32x4 mfma16(bf16x4 a, bf16x4 b, f32x4 c) { return __builtin_amdgcn_mfma_f32_16x16x16bf16_1k(a, b, c, 0, 0, 0); }

__device__ __forceinline__ float rows_max(float v) {
  auto a = __builtin_amdgcn_permlane16_swap(__float_as_uint(v), __float_as_uint(v), false, false);
  v = fmaxf(__uint_as_float(a[0]), __uint_as_float(a[1]));
  auto b = __builtin_amdgcn_permlane32_swap(__float_as_uint(v), __float_as_uint(v), false, false);
  return fmaxf(__uint_as_float(b[0]), __uint_as_float(b[1]));
}
__device__ __forceinline__ float rows_sum(float v) {
  auto a = __builtin_amdgcn_permlane16_swap(__float_as_uint(v), __float_as_uint(v), false, false);
  v = __uint_as_float(a[0]) + __uint_as_float(a[1]);
  auto b = __builtin_amdgcn_permlane32_swap(__float_as_uint(v), __float_as_uint(v), false, false);
  return __uint_as_float(b[0]) + __uint_as_float(b[1]);
}
__device__ __forceinline__ float row16_sum(float v) {
  v += __int_as_float(__builtin_amdgcn_update_dpp(0, __float_as_int(v), 0xB1, 0xf, 0xf, false));
  v += __int_as_float(__builtin_amdgcn_update_dpp(0, __float_as_int(v), 0x4E, 0xf, 0xf, false));
  v += __int_as_float(__builtin_amdgcn_update_dpp(0, __float_as_int(v), 0x141, 0xf, 0xf, false));
  v += __int_as_float(__builtin_amdgcn_update_dpp(0, __float_as_int(v), 0x140, 0xf, 0xf, false));
  return v;
}
__device__ __forceinline__ float gelu_tanh(float x) {
  const float c1 = -2.0f * 0.7978845608028654f * 1.4426950408889634f;
  const float c3 = c1 * 0.044715f;
  float t = x * (c1 + c3 * (x * x));
  return x * __builtin_amdgcn_rcpf(1.0f + __builtin_amdgcn_exp2f(t));
}
__device__ __forceinline__ f32x4 gelu4(f32x4 v) { return (f32x4){gelu_tanh(v[0]), gelu_tanh(v[1]), gelu_tanh(v[2]), gelu_tanh(v[3])}; }
__device__ __forceinline__ u32x4 swap_pair(u32x2 a, u32x2 b) {
  auto x = __builtin_amdgcn_permlane16_swap(a.x, b.x, false, false);
  auto y = __builtin_amdgcn_permlane16_swap(a.y, b.y, false, false);
  u32x4 r; r.x = x[0]; r.y = y[0]; r.z = x[1]; r.w = y[1];
  return r;
}
__device__ __forceinline__ u32x2 pk4(f32x4 v) { u32x2 r; r.x = pk_bf16(v[0], v[1]); r.y = pk_bf16(v[2], v[3]); return r; }
__device__ __forceinline__ u64 ldflag(const char* ws, unsigned off) { return __hip_atomic_load((GAS u64*)(ws + off), RLX_AGENT); }
__device__ __forceinline__ void poll_flags(const char* ws, unsigned off, u64 tag) {
#pragma unroll 1
  for (unsigned spins = 0; spins < (1u << 20); ++spins) {
    if (__all(ldflag(ws, off) == tag)) break;
    __builtin_amdgcn_s_sleep(1);
  }
}
template <int N> using IC = std::integral_constant<int, N>;
template <int N, class F> __device__ __forceinline__ void sfor(F f) {
  if constexpr (N > 0) { sfor<N - 1>(f); f(IC<N - 1>{}); }
}

constexpr int PA_BUF = 53248;
constexpr int PA_ZOFF = 43008;
constexpr int PA_BOFF = 18432;
constexpr int PB_BUF = 3 * SLOT;
#define ROTWB 3
#define ROTW1 3

__global__ __launch_bounds__(512, 2) void gm_fused(const __hip_bfloat16* x, const __hip_bfloat16* wqkv, const __hip_bfloat16* wo,
                                                   const __hip_bfloat16* win, const __hip_bfloat16* wsp, const __hip_bfloat16* bsp,
                                                   const __hip_bfloat16* wout, const __hip_bfloat16* gw, char* ws, __hip_bfloat16* out) {
  extern __shared__ __attribute__((aligned(16))) char smem_[];
  STAMP(0);
  lds_char* smem = (lds_char*)smem_;
  const unsigned lds = (unsigned)(size_t)smem;
  const int tid = threadIdx.x;
  const int wid = __builtin_amdgcn_readfirstlane(tid >> 6), lane = tid & 63, fr = lane & 15, fq = lane >> 4;
  const int xcd = blockIdx.x & 7, idx = blockIdx.x >> 3;
  const int tt = xcd * 4 + (idx & 3), fj = idx >> 2, nj = fj;
  const int rot2 = (fj + idx) & 3;
  const int g = fq, q4 = (lane & 15) >> 2, p4 = lane & 3;
  const unsigned tr_e = q4 * 64 + (2 * (g & 1) + (p4 >> 1)) * 16 + (p4 & 1) * 8;
  const unsigned tr_o = q4 * 64 + (2 * (1 - (g & 1)) + (p4 >> 1)) * 16 + (p4 & 1) * 8;
  const unsigned sw8 = ((lane & 7) ^ (lane >> 3)) * 16;
  rsrc_t rx = mk_rsrc(x, S_ * D_ * 2);
  rsrc_t rsp = mk_rsrc(wsp, S_ * S_ * 2);
  const unsigned sp_off = (unsigned)((tt * 64 + wid * 8 + (lane >> 3)) * 4096) + sw8;
  auto pf_sp = [&](int s) { glds16(rsp, smem + s * SLOT + wid * 1024, sp_off, P2IT(s >> 1) * 256 + (s & 1) * 128); };
  unsigned len; u64 pc;
  asm volatile("s_getpc_b64 %0\n gm_code_a:\n s_mov_b32 %1, gm_code_b - gm_code_a" : "=s"(pc), "=s"(len));
  unsigned wm0 = 0;
  auto warm = [&]() {
    if (WARM == 0) return;
    const bool code = WARM == 1 || wid < 5;
    const u64 base = code ? pc : (wid == 5 ? (u64)ws : (wid == 6 ? (u64)wout : (u64)wo));
    const unsigned size = code ? len : (wid == 5 ? (unsigned)(5 * MB4) : 4096u);
    const unsigned vo = code ? (unsigned)((wid * 64 + lane) * 128) : (unsigned)((lane >> 1) * MB4 + (lane & 1) * (MB4 - 256));
    wm0 = __builtin_amdgcn_raw_buffer_load_b32(mk_rsrc((const void*)base, size), vo, 0, 0);
  };
  u64 tag, fz_a;
  unsigned myflag;
  constexpr int LB = 6;
  const int ftb = fj < 3 ? fj * 6 : 18 + (fj - 3) * 5;
  const int nft = fj < 3 ? 6 : 5;
  const int fh = wid & 1, kg = wid >> 1;
  const int ft0 = ftb + fh * 3;
  const bool has2 = fh * 3 + 2 < nft;
  rsrc_t rz2 = mk_rsrc(ws + WS_Z2, NFT * 64 * 1024);
  const unsigned zb_off = (unsigned)((ft0 * 64 + kg) * 1024 + lane * 16);
  const unsigned zb_n2 = has2 ? 2 * 65536u : 65536u;
  const unsigned f2_off = (unsigned)(WS_F2 + xcd * 1024 + ((lane >> 1) * 4 + ((lane & 1) ? (ftb + nft - 1) / 11 : ftb / 11)) * 8);
  u32x4 bq[LB][3];
  auto issueB = [&](int it) {
    const unsigned so = (unsigned)(P2IT(it) * 4096);
    bq[it % LB][0] = ld16_sc1(rz2, zb_off, so); bq[it % LB][1] = ld16_sc1(rz2, zb_off, so + 65536u); bq[it % LB][2] = ld16_sc1(rz2, zb_off, so + zb_n2);
  };
  auto p2_first = [&]() {
#pragma unroll
    for (int it = 0; it < LB; ++it) issueB(it);
    FENCE();
  };
  bool early;

  if (xcd < 4) {
    const int panel = (xcd & 1) * 8 + (idx & 7), ct = (xcd >> 1) * 4 + (idx >> 3);
    const int rot1 = (idx & 7) + 2 * (idx >> 3);
    rsrc_t rw = mk_rsrc(wqkv, D_ * NQKV * 2);
    const int row0 = panel * 128 - 16;
    auto a_off = [&](int p) -> unsigned { int gr = row0 + 8 * p + (lane >> 3); gr = gr < 0 ? 0 : gr; return (unsigned)(gr * 1024) + sw8; };
    const unsigned v0 = a_off(wid), v1 = a_off(wid + 8), v5 = a_off(wid + 10);
    const unsigned v2 = (unsigned)((wid * 8 + ((lane & 31) >> 2)) * (NQKV * 2) + (ct * 64 + (lane >> 5) * 32 + (((lane & 3) ^ (2 * (wid & 1))) * 8)) * 2);
    rsrc_t rwz = mk_rsrc(win, D_ * F_ * 2);
    const unsigned vz = (unsigned)((wid * 8 + ((lane & 31) >> 2)) * (F_ * 2) + (ct * 64 + (lane >> 5) * 32 + (((lane & 3) ^ (2 * (wid & 1))) * 8)) * 2);
    lds_char* const dA = smem + wid * 1024; lds_char* const dB = smem + PA_BOFF + wid * 3072;
    auto LD = [&](int i, int u) {
      const int kt = (u & ~ROTW1) + ((u + rot1) & ROTW1);
      const int bo = (u % 3) * PA_BUF;
      if (i == 0) glds16(rx, dA + bo, v0, kt * 128);
      if (i == 1) glds16(rw, dB + bo, v2, kt * (64 * NQKV * 2));
      if (i == 2) glds16(rx, dA + bo + 8192, v1, kt * 128);
      if (i == 3) glds16(rw, dB + bo + 1024, v2, kt * (64 * NQKV * 2) + 1024);
      if (i == 4) glds16(rw, dB + bo + 2048, v2, kt * (64 * NQKV * 2) + 2048);
      if (i == 5) glds16(rwz, dA + bo + PA_ZOFF, vz, kt * (64 * F_ * 2));
      if (i == 6 && wid >= 6) glds16(rx, dA + bo + 10240, v5, kt * 128);
    };
    const int grow = panel * 128 + ct * 16 + wid * 2;
    u32x4 gx0, gx1, gv;
#pragma unroll
    for (int u = 0; u < 2; ++u) {
#pragma unroll
      for (int i = 0; i < 7; ++i) LD(i, u);
      if (u == 0) BAR();
      FENCE();
    }
    STAMP(1);
    const int hw = wid & 3, half = wid >> 2;
    const unsigned a_rd0 = lds + (half * 64 + fr) * 128 + ((fq ^ (fr & 7)) * 16), a_rd1 = a_rd0 ^ 64u;
    const unsigned b_rd = lds + PA_BOFF + g * 3072 + ((hw & 1) ? tr_o : tr_e) + (hw >> 1) * 512;
    const unsigned z_rd = lds + PA_ZOFF + g * 1024 + ((hw & 1) ? tr_o : tr_e) + (hw >> 1) * 512;
    f32x4 acc[18];
#pragma unroll
    for (int i = 0; i < 18; ++i) acc[i] = (f32x4){0.f, 0.f, 0.f, 0.f};
    struct Set { bf16x8 a[2][5]; bf16x4 l[2][4], h[2][4]; };
    Set X, Y;
    auto rdg = [&](Set& s, auto U, auto J) {
      constexpr int u = decltype(U)::value, j = decltype(J)::value, ks = j / 9, i = j % 9;
      constexpr int bo = (u % 3 == 1) ? PA_BUF : 0;
      const unsigned aa = (ks ? a_rd1 : a_rd0) + (u % 3 == 2 ? 2u * PA_BUF : 0u), bb = b_rd + (u % 3) * (unsigned)PA_BUF, zz = z_rd + (u % 3) * (unsigned)PA_BUF;
      if constexpr (i == 0) tr_frag<ks * 12288>(s.l[ks][0], s.h[ks][0], bb);
      else if constexpr (i <= 4) rd128<bo + i * 2048>(s.a[ks][i], aa);
      else if constexpr (i == 5) tr_frag<ks * 4096>(s.l[ks][3], s.h[ks][3], zz);
      else if constexpr (i == 6) tr_frag<ks * 12288 + 1024>(s.l[ks][1], s.h[ks][1], bb);
      else if constexpr (i == 7) rd128<bo>(s.a[ks][0], aa);
      else tr_frag<ks * 12288 + 2048>(s.l[ks][2], s.h[ks][2], bb);
    };
    WAIT_V(6); BAR();
    STAMP(2);
    sfor<18>([&](auto J) { rdg(X, IC<0>{}, J); });
    sfor<8>([&](auto U) {
      constexpr int u = decltype(U)::value;
      Set& c = (u & 1) ? Y : X; Set& n = (u & 1) ? X : Y;
      WAIT_L(0);
      WAIT_V(u == 0 ? 0 : (u == 2 ? 10 : (u < 6 ? 6 : 5))); BAR();
#pragma unroll
      for (int ks = 0; ks < 2; ++ks) {
#pragma unroll
        for (int i = 0; i < 5; ++i) PIN(c.a[ks][i]);
#pragma unroll
        for (int i = 0; i < 4; ++i) { PIN(c.l[ks][i]); PIN(c.h[ks][i]); }
      }
      if constexpr (u == 7) { fz_a = ldflag(ws, f2_off); SCHED(); }
      sfor<36>([&](auto J) {
        constexpr int j = decltype(J)::value, ks = j / 18, i = j % 18;
        if constexpr (i < 4) acc[i] = mfma32(cat8(c.l[ks][0], c.h[ks][0]), c.a[ks][i + 1], acc[i]);
        else if constexpr (i < 8) acc[10 + i] = mfma32(cat8(c.l[ks][3], c.h[ks][3]), c.a[ks][i - 3], acc[10 + i]);
        else if constexpr (i < 13) acc[i - 4] = mfma32(cat8(c.l[ks][1], c.h[ks][1]), c.a[ks][i - 8], acc[i - 4]);
        else acc[i - 4] = mfma32(c.a[ks][i - 13], cat8(c.l[ks][2], c.h[ks][2]), acc[i - 4]);
        SCHED();
        if constexpr (j % 5 == 0) {
          constexpr int k = j / 5;
          if constexpr (u == 0) { if constexpr (k < 7) LD(k < 6 ? k : 6, 2); }
          else if constexpr (u < 5) { if constexpr (k < 6) LD(k, u + 3); }
          else if constexpr (u == 5) { if constexpr (k < 5) pf_sp(13 + k); }
          else if constexpr (u == 6) { if constexpr (k < 6) pf_sp(k); }
        }
        if constexpr (u == 0 && j >= 30) LD(j - 30, 3);
        if constexpr (u == 1 && j == 33) {
          warm();
          tag = ldflag(ws, WS_EP);
          gx0 = *(const u32x4*)((const char*)x + (size_t)grow * 1024 + lane * 16);
          gx1 = *(const u32x4*)((const char*)x + (size_t)grow * 1024 + 1024 + lane * 16);
          gv = *(const u32x4*)((const char*)gw + lane * 16);
        }
        constexpr int r = j - (j + 4) / 5;
        if constexpr (u < 7 && j % 5 != 0 && r % 4 != 3 && r - r / 4 < 18) rdg(n, IC<u + 1>{}, IC<r - r / 4>{});
        SCHED();
      });
      if constexpr (u < 5) LD(6, u + 3);
    });
    STAMP(3);
    PIN(tag); tag += 1ull;
    early = __all(fz_a == tag);
    bf16x4 qb[4], kb[5], vb[5];
#pragma unroll
    for (int i = 0; i < 4; ++i) qb[i] = __builtin_bit_cast(bf16x4, pk4(acc[i]));
#pragma unroll
    for (int i = 0; i < 5; ++i) { kb[i] = __builtin_bit_cast(bf16x4, pk4(acc[4 + i])); vb[i] = __builtin_bit_cast(bf16x4, pk4(acc[9 + i])); }
    const int par = fr & 1;
    const int j0 = 4 * fq + par, j1 = j0 + 2;
    const bool v00 = j0 >= fr + 2, v01 = j1 >= fr + 2, v10 = j0 <= fr, v11 = j1 <= fr;
    const float sc = 0.25f * 1.4426950408889634f, NEG = -3.0e38f;
    u32x2 ov[4];
#pragma unroll
    for (int i = 0; i < 4; ++i) {
      f32x4 z4 = {0.f, 0.f, 0.f, 0.f};
      f32x4 s0 = mfma16(kb[i], qb[i], z4);
      f32x4 s1 = mfma16(kb[i + 1], qb[i], z4);
      float a0 = par ? s0[1] : s0[0], a1 = par ? s0[3] : s0[2];
      float b0 = par ? s1[1] : s1[0], b1 = par ? s1[3] : s1[2];
      const bool prev_ok = !(i == 0 && half == 0 && panel == 0);
      const bool m00 = v00 && prev_ok, m01 = v01 && prev_ok;
      a0 = m00 ? a0 : NEG; a1 = m01 ? a1 : NEG; b0 = v10 ? b0 : NEG; b1 = v11 ? b1 : NEG;
      float mx = rows_max(fmaxf(fmaxf(a0, a1), fmaxf(b0, b1)));
      float e00 = m00 ? __builtin_amdgcn_exp2f((a0 - mx) * sc) : 0.f;
      float e01 = m01 ? __builtin_amdgcn_exp2f((a1 - mx) * sc) : 0.f;
      float e10 = v10 ? __builtin_amdgcn_exp2f((b0 - mx) * sc) : 0.f;
      float e11 = v11 ? __builtin_amdgcn_exp2f((b1 - mx) * sc) : 0.f;
      float inv = __builtin_amdgcn_rcpf(rows_sum((e00 + e01) + (e10 + e11)));
      u32x4 pw;
      const unsigned sh = par * 16;
      pw.x = (pk_bf16(e00 * inv, 0.f) & 0xffffu) << sh;
      pw.y = (pk_bf16(e01 * inv, 0.f) & 0xffffu) << sh;
      pw.z = (pk_bf16(e10 * inv, 0.f) & 0xffffu) << sh;
      pw.w = (pk_bf16(e11 * inv, 0.f) & 0xffffu) << sh;
      ov[i] = pk4(mfma32(cat8(vb[i], vb[i + 1]), __builtin_bit_cast(bf16x8, pw), z4));
      if (early && i < 2) issueB(i);
    }
    u32x4 zst[2];
#pragma unroll
    for (int i = 0; i < 2; ++i) {
      const u32x2 a = pk4(gelu4(acc[14 + 2 * i])), b = pk4(gelu4(acc[15 + 2 * i]));
      zst[i] = (u32x4){a.x, a.y, b.x, b.y};
    }
    float s0 = 0.f, s1 = 0.f;
#pragma unroll
    for (int i = 0; i < 4; ++i) {
      s0 += bf_lo(gx0[i]) * bf_lo(gv[i]) + bf_hi(gx0[i]) * bf_hi(gv[i]);
      s1 += bf_lo(gx1[i]) * bf_lo(gv[i]) + bf_hi(gx1[i]) * bf_hi(gv[i]);
    }
    s0 = rows_sum(row16_sum(s0)); s1 = rows_sum(row16_sum(s1));
    float gt = __builtin_amdgcn_rcpf(1.0f + __builtin_amdgcn_exp2f(-(lane == 0 ? s0 : s1) * 1.4426950408889634f));
    u32x4 ost0 = swap_pair(ov[0], ov[1]), ost1 = swap_pair(ov[2], ov[3]);
    PIN(ost0); PIN(ost1); PIN(zst[0]); PIN(zst[1]); PIN(gt);
    SCHED();
    const int h = ct * 4 + hw;
    rsrc_t ro = mk_rsrc(ws + WS_OF, 128 * 16 * 1024);
    rsrc_t rz1 = mk_rsrc(ws + WS_Z1, 64 * NFT * 1024);
    {
      const unsigned oo = (unsigned)(((panel * 8 + half * 4 + (fq & 1)) * 16 + (h >> 1)) * 1024 + ((2 * (h & 1) + (fq >> 1)) * 16 + fr) * 16);
      st16_sc1(ro, oo, ost0); st16_sc1(ro, oo + 2 * 16384, ost1);
      const unsigned zo = (unsigned)(((panel * 4 + half * 2) * NFT + ct * 4 + hw) * 1024 + lane * 16);
      st16_sc1(rz1, zo, zst[0]); st16_sc1(rz1, zo + NFT * 1024, zst[1]);
    }
    if (lane < 2) __hip_atomic_store((GAS unsigned*)(ws + WS_GT) + grow + lane, __float_as_uint(gt), RLX_AGENT);
    myflag = (unsigned)(WS_FA + (panel * 8 + ct) * 8);
  } else {
    const int r64 = (xcd & 1) * 16 + (idx & 15), zj = ((xcd >> 1) & 1) * 2 + (idx >> 4);
    constexpr int pitchW = F_ * 2;
    rsrc_t rw = mk_rsrc(win, D_ * pitchW);
    const unsigned ox = (unsigned)((r64 * 64 + wid * 8 + (lane >> 3)) * 1024) + sw8;
    auto b_off = [&](int i) -> unsigned {
      int lc = i * 64 + (lane >> 5) * 32 + (((lane & 3) ^ (2 * (wid & 1))) * 8);
      if (lc >= (zj == 3 ? 160 : 176)) lc -= (zj == 3 ? 32 : 16);
      return (unsigned)((wid * 8 + ((lane & 31) >> 2)) * pitchW + (zj * 176 + lc) * 2);
    };
    const unsigned ob0 = b_off(0), ob1 = b_off(1), ob2 = b_off(2);
    int lz = 512 + zj * 48 + (lane >> 5) * 32 + (((lane & 3) ^ (2 * (wid & 1))) * 8); lz = lz >= FH ? lz - 32 : lz;
    const unsigned oz = (unsigned)((wid * 8 + ((lane & 31) >> 2)) * pitchW + lz * 2);
    auto KT = [&](int v) { return (v & 7 & ~ROTWB) + ((v + idx) & ROTWB); };
    auto issueW = [&](int i, int v) {
      const int kt = KT(v);
      const unsigned so = (unsigned)(kt * (64 * pitchW) + FH * 2);
      lds_char* d = smem + 8 * SLOT + (v & 3) * PB_BUF + wid * 3072;
      if (i == 0) glds16(rx, smem + v * SLOT + wid * 1024, ox, kt * 128);
      if (i == 1) glds16(rw, d, ob0, so);
      if (i == 2) glds16(rw, d + 1024, ob1, so);
      if (i == 3) glds16(rw, d + 2048, ob2, so);
    };
    auto issueZ = [&](int v) { glds16(rw, smem + (8 + v) * SLOT + wid * 1024, oz, KT(v) * (64 * pitchW)); };
#pragma unroll
    for (int v = 0; v < 3; ++v) {
#pragma unroll
      for (int i = 0; i < 4; ++i) issueW(i, v);
      if (v == 0) BAR();
      FENCE();
    }
    STAMP(1);
    const int mp = wid & 1, nq = wid >> 1;
    const unsigned a_rd0 = lds + (mp * 32 + fr) * 128 + ((fq ^ (fr & 7)) * 16), a_rd1 = a_rd0 ^ 64u;
    unsigned b_rd[3];
#pragma unroll
    for (int n = 0; n < 3; ++n) { const int t = nq * 3 + n; b_rd[n] = lds + 8 * SLOT + g * 3072 + ((t & 1) ? tr_o : tr_e) + (t >> 1) * 512; }
    f32x4 acc[2][3];
#pragma unroll
    for (int i = 0; i < 6; ++i) acc[i / 3][i % 3] = (f32x4){0.f, 0.f, 0.f, 0.f};
    struct Set { bf16x8 a[2][2]; bf16x4 l[2][3], h[2][3]; };
    Set X, Y;
    auto rdg = [&](Set& s, auto V, auto J) {
      constexpr int v = decltype(V)::value, j = decltype(J)::value, ks = j / 5, i = j % 5;
      constexpr int xo = (v & 7) * SLOT, wo_ = (v & 3) * PB_BUF;
      if constexpr (i == 0) rd128<xo>(s.a[ks][0], ks ? a_rd1 : a_rd0);
      else if constexpr (i == 2) rd128<xo + 2048>(s.a[ks][1], ks ? a_rd1 : a_rd0);
      else {
        constexpr int n = i == 1 ? 0 : i - 2;
        if constexpr (wo_ < 2 * PB_BUF) tr_frag<wo_ + ks * 12288>(s.l[ks][n], s.h[ks][n], b_rd[n]);
        else tr_frag<wo_ - 2 * PB_BUF + ks * 12288>(s.l[ks][n], s.h[ks][n], b_rd[n] + 2 * PB_BUF);
      }
    };
    const int nval = (zj == 3 ? 10 : 11) - nq * 3;
    rsrc_t rst = mk_rsrc(ws + WS_ST, 2048 * 64);
    const unsigned goff = (unsigned)((r64 * 64 + mp * 32 + (lane >> 1)) * 64 + (lane & 1) * 32);
    WAIT_V(8); BAR();
    STAMP(2);
    sfor<10>([&](auto J) { rdg(X, IC<0>{}, J); });
    sfor<8>([&](auto V) {
      constexpr int v = decltype(V)::value;
      Set& c = (v & 1) ? Y : X; Set& n = (v & 1) ? X : Y;
      constexpr int wv[8] = {4, 8, 8, 8, 8, 7, 6, 9};
      WAIT_L(0);
      WAIT_V(wv[v]); BAR();
#pragma unroll
      for (int ks = 0; ks < 2; ++ks) {
        PIN(c.a[ks][0]); PIN(c.a[ks][1]);
#pragma unroll
        for (int i = 0; i < 3; ++i) { PIN(c.l[ks][i]); PIN(c.h[ks][i]); }
      }
      sfor<12>([&](auto J) {
        constexpr int j = decltype(J)::value, ks = j / 6, nn = (j % 6) >> 1, m = j & 1;
        acc[m][nn] = mfma32(c.a[ks][m], cat8(c.l[ks][nn], c.h[ks][nn]), acc[m][nn]);
        SCHED();
        if constexpr (j % 3 == 0) {
          constexpr int k = j / 3;
          if constexpr (v == 0) { issueW(k, 3); }
          else if constexpr (v < 4) issueW(k, v + 4);
          else if constexpr (v < 7) { if constexpr (k < 3 && (v - 4) * 3 + k < 8) issueZ((v - 4) * 3 + k); }
        }
        if constexpr (v < 7 && j < 10) rdg(n, IC<v + 1>{}, J);
        if constexpr (v == 0 && j >= 10) { issueW(2 * (j - 10), 4); issueW(2 * (j - 10) + 1, 4); }
        SCHED();
      });
      if constexpr (v == 1) { warm(); tag = ldflag(ws, WS_EP); FENCE(); }
    });
    STAMP(4);
    PIN(tag); tag += 1ull;
    {
      const float w1 = nval > 1 ? 1.f : 0.f, w2 = nval > 2 ? 1.f : 0.f;
      const float icnt = nval > 2 ? (1.f / 48.f) : (nval > 1 ? (1.f / 32.f) : (1.f / 16.f));
      LAS f32x2* tab = (LAS f32x2*)(smem + 8 * SLOT + 3 * PB_BUF);
#pragma unroll
      for (int m = 0; m < 2; ++m) {
#pragma unroll
        for (int nn = 0; nn < 3; ++nn) acc[m][nn] = gelu4(acc[m][nn]);
        const f32x4 z4 = {0.f, 0.f, 0.f, 0.f};
        const f32x4 a1 = nval > 1 ? acc[m][1] : z4, a2 = nval > 2 ? acc[m][2] : z4;
        f32x4 sm = acc[m][0] + a1 + a2, mean, m2;
#pragma unroll
        for (int r = 0; r < 4; ++r) mean[r] = row16_sum(sm[r]) * icnt;
        const f32x4 d0 = acc[m][0] - mean, d1 = (a1 - mean) * w1, d2 = (a2 - mean) * w2;
        sm = d0 * d0 + d1 * d1 + d2 * d2;
#pragma unroll
        for (int r = 0; r < 4; ++r) m2[r] = row16_sum(sm[r]);
        if (fr < 4) {
          const float mv = fr == 0 ? mean[0] : (fr == 1 ? mean[1] : (fr == 2 ? mean[2] : mean[3]));
          const float qv = fr == 0 ? m2[0] : (fr == 1 ? m2[1] : (fr == 2 ? m2[2] : m2[3]));
          tab[(mp * 32 + m * 16 + 4 * fq + fr) * 4 + nq] = (f32x2){mv, qv};
        }
      }
      WAIT_L(0); WAIT_V(0); BAR();
      if (wid == 7) {
        const f32x4 p01 = *(LAS f32x4*)(tab + lane * 4), p23 = *(LAS f32x4*)(tab + lane * 4 + 2);
        const float n3 = zj == 3 ? 16.f : 32.f, nt = 144.f + n3;
        const float mu = (48.f * (p01[0] + p01[2] + p23[0]) + n3 * p23[2]) / nt;
        const float d0 = p01[0] - mu, d1 = p01[2] - mu, d2 = p23[0] - mu, d3 = p23[2] - mu;
        const float q = (p01[1] + p01[3]) + (p23[1] + p23[3]) + 48.f * (d0 * d0 + d1 * d1 + d2 * d2) + n3 * d3 * d3;
        st16_sc1(rst, (unsigned)((r64 * 64 + lane) * 64 + zj * 16), (u32x4){__float_as_uint(mu), (unsigned)tag, __float_as_uint(q), (unsigned)(tag >> 32)});
      }
    }
    STAMP(5);
    if (nq < 3 && zj * 3 + nq < 11) {
      const unsigned z_rd = lds + 8 * SLOT + g * 1024 + ((nq & 1) ? tr_o : tr_e) + (nq >> 1) * 512;
      f32x4 az[2] = {(f32x4){0.f, 0.f, 0.f, 0.f}, (f32x4){0.f, 0.f, 0.f, 0.f}};
      sfor<4>([&](auto Q) {
        constexpr int q = decltype(Q)::value;
        bf16x8 a[4][2]; bf16x4 l[4], h[4];
        sfor<4>([&](auto K) {
          constexpr int k = decltype(K)::value, v = q * 2 + (k >> 1), ks = k & 1;
          rd128<v * SLOT>(a[k][0], ks ? a_rd1 : a_rd0); rd128<v * SLOT + 2048>(a[k][1], ks ? a_rd1 : a_rd0);
          tr_frag<(v & 3) * SLOT + ks * 4096>(l[k], h[k], z_rd + (v >> 2) * 4 * SLOT);
        });
        WAIT_L(0);
#pragma unroll
        for (int k = 0; k < 4; ++k) {
          PIN(a[k][0]); PIN(a[k][1]); PIN(l[k]); PIN(h[k]);
          az[0] = mfma32(cat8(l[k], h[k]), a[k][0], az[0]); az[1] = mfma32(cat8(l[k], h[k]), a[k][1], az[1]);
        }
      });
      rsrc_t rz1 = mk_rsrc(ws + WS_Z1, 64 * NFT * 1024);
      const u32x2 a = pk4(gelu4(az[0])), b = pk4(gelu4(az[1]));
      st16_sc1(rz1, (unsigned)(((r64 * 2 + mp) * NFT + 32 + zj * 3 + nq) * 1024 + lane * 16), (u32x4){a.x, a.y, b.x, b.y});
    }
    STAMP(3);
    {
      const unsigned tlo = (unsigned)tag, thi = (unsigned)(tag >> 32);
      u32x4 pt0, pt1;
#pragma unroll 1
      for (unsigned spins = 0; spins < (1u << 20); ++spins) {
        pt0 = ld16_sc1(rst, goff, 0); pt1 = ld16_sc1(rst, goff, 16);
        if (__all(pt0.y == tlo && pt0.w == thi && pt1.y == tlo && pt1.w == thi)) break;
        __builtin_amdgcn_s_sleep(4);
      }
      const float a0 = __uint_as_float(pt0.x), a1 = __uint_as_float(pt1.x), n1 = (lane & 1) ? 160.f : 176.f;
      float sm = 176.f * a0 + n1 * a1;
      sm += __int_as_float(__builtin_amdgcn_update_dpp(0, __float_as_int(sm), 0xB1, 0xf, 0xf, false));
      const float mu = sm * (1.0f / 688.f), d0 = a0 - mu, d1 = a1 - mu;
      float q = __uint_as_float(pt0.z) + __uint_as_float(pt1.z) + 176.f * d0 * d0 + n1 * d1 * d1;
      q += __int_as_float(__builtin_amdgcn_update_dpp(0, __float_as_int(q), 0xB1, 0xf, 0xf, false));
      const float rstd = 1.0f / sqrtf(q * (1.0f / 688.f) + 1e-5f);
      LAS float* tb = (LAS float*)(smem + 19 * SLOT + wid * 1024);
      tb[(lane & 1) * 32 + (lane >> 1)] = (lane & 1) ? -mu * rstd : rstd;
      WAIT_L(0);
      const f32x4 r0 = *(LAS f32x4*)(tb + 4 * fq), r1 = *(LAS f32x4*)(tb + 16 + 4 * fq);
      const f32x4 c0 = *(LAS f32x4*)(tb + 32 + 4 * fq), c1 = *(LAS f32x4*)(tb + 48 + 4 * fq);
      const int q2 = 2 * (fq & 1) + (fq >> 1);
#pragma unroll
      for (int nn = 0; nn < 3; ++nn)
        if (nn < nval)
          st16_sc1(rz2, (unsigned)(((zj * 11 + nq * 3 + nn) * 64 + r64 * 2 + mp) * 1024 + (q2 * 16 + fr) * 16),
                   swap_pair(pk4(acc[0][nn] * r0 + c0), pk4(acc[1][nn] * r1 + c1)));
    }
    early = false;
    myflag = (unsigned)(WS_F2 + (r64 * 4 + zj) * 8);
  }
  STAMP(6);
  WAIT_V(0); BAR();
  asm volatile("" :: "v"(wm0));
  if (tid < (xcd < 4 ? 1 : 8)) __hip_atomic_store((GAS u64*)(ws + myflag + tid * 1024), tag, RLX_AGENT);
  STAMP(7);
  if (xcd >= 4) {
#pragma unroll
    for (int s = 0; s < 6; ++s) pf_sp(s);
  }
#pragma unroll
  for (int s = 6; s < 13; ++s) pf_sp(s);
  if (xcd >= 4) {
#pragma unroll
    for (int s = 13; s < 18; ++s) pf_sp(s);
    WAIT_V(12);
  }
  if (wid == 0 && !early) poll_flags(ws, f2_off, tag);
  BAR();
  STAMP(8);
  if (!early) { issueB(0); issueB(1); }
  FENCE();

  rsrc_t rwt = mk_rsrc(wout, FH * D_ * 2);
  const unsigned wt_off = (unsigned)((wid * 8 + ((lane & 31) >> 2)) * 1024 + (nj * 64 + (lane >> 5) * 32 + (((lane & 3) ^ (2 * (wid & 1))) * 8)) * 2);
  const int t16 = tt * 4 + kg;
  const int mw = wid & 3, kp = wid >> 2, t16a = tt * 4 + mw;
  f32x4 acc[4][3];
#pragma unroll
  for (int i = 0; i < 12; ++i) acc[i / 3][i % 3] = (f32x4){0.f, 0.f, 0.f, 0.f};
  const unsigned a_rd = lds + (kg >> 1) * SLOT + fr * 128 + (((4 * (kg & 1) + fq) ^ (fr & 7)) * 16);
  bf16x8 XA[4], YA[4];
  auto rd2 = [&](bf16x8 (&a)[4], auto IT, auto M) {
    constexpr int it = decltype(IT)::value, m = decltype(M)::value, off = (it % 9) * 2 * SLOT + m * 2048;
    if constexpr (off < 65536) rd128<off>(a[m], a_rd); else if constexpr (off < 131072) rd128<off - 65536>(a[m], a_rd + 65536u); else rd128<off - 131072>(a[m], a_rd + 131072u);
  };
  u64 f1v = 0; u32x2 z1v[3]; float bias = 0.f, gate = 0.f;
  u32x4 oq[8];
  rsrc_t rwo = mk_rsrc(wo, D_ * D_ * 2), rof = mk_rsrc(ws + WS_OF, 128 * 16 * 1024);
  const unsigned f1_off = (lane & 8) ? (unsigned)(WS_F2 + xcd * 1024 + (tt * 4 + (lane & 3)) * 8) : (unsigned)(WS_FA + ((tt >> 1) * 8 + (lane & 7)) * 8);
  STAMP(9);
  sfor<4>([&](auto M) { rd2(XA, IC<0>{}, M); });
  sfor<16>([&](auto IT) {
    constexpr int it = decltype(IT)::value;
    bf16x8 (&c)[4] = (it & 1) ? YA : XA; bf16x8 (&n)[4] = (it & 1) ? XA : YA;
    WAIT_L(0);
    if constexpr (it == 2) { WAIT_V(12); BAR(); }
    if constexpr (it == 8) WAIT_V(9);
    if constexpr (it == 12) WAIT_V(3);
    if constexpr (it == 4 || it == 8 || it == 12 || it == 14) BAR();
    SCHED();
#pragma unroll
    for (int i = 0; i < 4; ++i) PIN(c[i]);
    sfor<12>([&](auto J) {
      constexpr int j = decltype(J)::value, nn = j >> 2, m = j & 3;
      acc[m][nn] = mfma32(__builtin_bit_cast(bf16x8, bq[it % LB][nn]), c[m], acc[m][nn]);
      SCHED();
      if constexpr (it < 15 && j % 2 == 0 && j < 8) rd2(n, IC<it + 1>{}, IC<j / 2>{});
      if constexpr ((it == 4 || it == 5 || it == 6 || it == 8) && (j == 1 || j == 3 || j == 5 || j == 7)) {
        constexpr int sl = (it == 8 ? 10 : (it - 4) * 4) + (j >> 1);
        if constexpr (!(it == 6 && j > 3))
          glds16(rsp, smem + sl * SLOT + wid * 1024, sp_off, P2IT((sl >> 1) + 9) * 256 + (sl & 1) * 128);
      }
      if constexpr (it >= 12 && it < 14 && (j == 1 || j == 3 || j == 5 || j == 7)) {
        constexpr int kt = (it - 12) * 4 + (j >> 1);
        glds16(rwo, smem + kt * SLOT + wid * 1024, wt_off, kt * 65536);
      }
      if constexpr (it == 14 && (j == 1 || j == 3 || j == 5)) {
        constexpr int kt = j >> 1;
        glds16(rwt, smem + (8 + kt) * SLOT + wid * 1024, wt_off, kt * 65536);
      }
      if constexpr (it < 2 && j % 2 == 0)
        bq[2 + it * 2 + j / 6][(j / 2) % 3] = ld16_sc1(rz2, zb_off, (unsigned)(P2IT(2 + it * 2 + j / 6) * 4096) + ((j / 2) % 3 == 0 ? 0u : ((j / 2) % 3 == 1 ? 65536u : zb_n2)));
      if constexpr (it == 15 && j < 8) oq[j] = ld16_sc1(rof, lane * 16, (t16a * 16 + j * 2 + kp) * 1024);
      if constexpr (j % 4 == 3 && it + LB < 16)
        bq[it % LB][nn] = ld16_sc1(rz2, zb_off, (unsigned)(P2IT(it + LB) * 4096) + (nn == 0 ? 0u : (nn == 1 ? 65536u : zb_n2)));
      SCHED();
    });
    if constexpr (it == 8) f1v = ldflag(ws, f1_off);
    if constexpr (it == 12) {
      if (!__all(f1v == tag)) poll_flags(ws, f1_off, tag);
      rsrc_t rz1 = mk_rsrc(ws + WS_Z1, 64 * NFT * 1024);
#pragma unroll
      for (int nn = 0; nn < 3; ++nn)
        z1v[nn] = __builtin_amdgcn_raw_buffer_load_b64(rz1, (unsigned)(((t16 >> 1) * NFT + ft0 + (nn < 2 || has2 ? nn : 0)) * 1024 + lane * 16 + (t16 & 1) * 8), 0, AUX_SC1);
      bias = __bfloat162float(bsp[t16 * 16 + fr]);
      gate = __uint_as_float(__hip_atomic_load((GAS unsigned*)(ws + WS_GT) + t16a * 16 + fr, RLX_AGENT));
    }
    SCHED();
  });
  STAMP(10);
  BAR();
  lds_char* red = smem + 11 * SLOT;
  rsrc_t ruf = mk_rsrc(ws + WS_UF, 128 * NUK * 1024);
  {
#pragma unroll
    for (int m = 0; m < 4; ++m)
      if (m != kg) {
        const int src = kg - (kg > m ? 1 : 0);
#pragma unroll
        for (int n = 0; n < 3; ++n) *(LAS f32x4*)(red + (((fh * 4 + m) * 3 + src) * 3 + n) * 1024 + lane * 16) = acc[m][n];
      }
    WAIT_L(0); BAR();
    u32x2 uv[3];
#pragma unroll
    for (int n = 0; n < 3; ++n) {
      f32x4 gs = (kg == 0 ? acc[0][n] : (kg == 1 ? acc[1][n] : (kg == 2 ? acc[2][n] : acc[3][n]))) + bias;
#pragma unroll
      for (int s = 0; s < 3; ++s) gs += *(LAS f32x4*)(red + (((fh * 4 + kg) * 3 + s) * 3 + n) * 1024 + lane * 16);
      uv[n].x = pk_bf16(bf_lo(z1v[n].x) * gs[0], bf_hi(z1v[n].x) * gs[1]);
      uv[n].y = pk_bf16(bf_lo(z1v[n].y) * gs[2], bf_hi(z1v[n].y) * gs[3]);
    }
    if (!has2) uv[2] = (u32x2){0u, 0u};
    {
      const int ft = ft0 + (fq & 1);
      st16_sc1(ruf, (unsigned)((t16 * NUK + (ft >> 1)) * 1024 + ((2 * (ft & 1) + (fq >> 1)) * 16 + fr) * 16), swap_pair(uv[0], uv[1]));
    }
    if (has2 || ft0 + 2 == NFT) {
      const int ft = ft0 + 2;
      const u32x4 w = swap_pair(uv[2], uv[2]);
      if (!(fq & 1)) st16_sc1(ruf, (unsigned)((t16 * NUK + (ft >> 1)) * 1024 + ((2 * (ft & 1) + (fq >> 1)) * 16 + fr) * 16), w);
    }
  }
  WAIT_L(0); BAR();
  STAMP(11);
  const unsigned w_rd_e = lds + (4 * kp + g) * 1024 + tr_e, w_rd_o = lds + (4 * kp + g) * 1024 + tr_o;
  f32x4 acca[4];
#pragma unroll
  for (int n = 0; n < 4; ++n) acca[n] = (f32x4){0.f, 0.f, 0.f, 0.f};
  {
#pragma unroll
    for (int k = 3; k < 11; ++k)
      glds16(rwt, smem + (8 + k) * SLOT + wid * 1024, wt_off - ((k == 10 && wid >= 6) ? 16384u : 0u), k * 65536);
    FENCE();
    WAIT_V(8); BAR();
    if (tid == 0) __hip_atomic_store((GAS u64*)(ws + WS_FU) + tt * 8 + fj, tag, RLX_AGENT);
    STAMP(12);
    bf16x4 bl[8][4], bh[8][4];
#pragma unroll
    for (int i = 0; i < 8; ++i) {
      const unsigned so = i * SLOT;
      tr_frag<0>(bl[i][0], bh[i][0], w_rd_e + so); tr_frag<0>(bl[i][1], bh[i][1], w_rd_o + so);
      tr_frag<512>(bl[i][2], bh[i][2], w_rd_e + so); tr_frag<512>(bl[i][3], bh[i][3], w_rd_o + so);
      if (i >= 1) {
        WAIT_L(8);
#pragma unroll
        for (int n = 0; n < 4; ++n) { PIN(bl[i - 1][n]); PIN(bh[i - 1][n]); acca[n] = mfma32(cat8(bl[i - 1][n], bh[i - 1][n]), __builtin_bit_cast(bf16x8, oq[i - 1]), acca[n]); }
        SCHED();
      }
    }
    WAIT_L(0);
#pragma unroll
    for (int n = 0; n < 4; ++n) { PIN(bl[7][n]); PIN(bh[7][n]); acca[n] = mfma32(cat8(bl[7][n], bh[7][n]), __builtin_bit_cast(bf16x8, oq[7]), acca[n]); }
    if (blockIdx.x == 0 && wid == 7) {
      poll_flags(ws, (unsigned)(WS_FA + lane * 8), tag); poll_flags(ws, (unsigned)(WS_FA + 512 + lane * 8), tag);
      poll_flags(ws, (unsigned)(WS_F2 + lane * 8), tag); poll_flags(ws, (unsigned)(WS_F2 + 512 + lane * 8), tag);
      if (lane == 0) __hip_atomic_store((GAS u64*)(ws + WS_EP), tag, RLX_AGENT);
    }
  }
  STAMP(13);
  {
    WAIT_V(0); BAR();
    bf16x4 wl[11][4], wh[11][4];
#pragma unroll
    for (int i = 0; i < 11; ++i) {
      const unsigned so = (8 + i) * SLOT;
      tr_frag<0>(wl[i][0], wh[i][0], w_rd_e + so); tr_frag<0>(wl[i][1], wh[i][1], w_rd_o + so);
      tr_frag<512>(wl[i][2], wh[i][2], w_rd_e + so); tr_frag<512>(wl[i][3], wh[i][3], w_rd_o + so);
      if ((i & 1) || i == 10) WAIT_L(0);
    }
    if (wid == 0) poll_flags(ws, (unsigned)(WS_FU + (tt * 8 + (lane & 7)) * 8), tag);
    BAR();
    STAMP(14);
    u32x4 aq[11];
#pragma unroll
    for (int i = 0; i < 11; ++i) aq[i] = ld16_sc1(ruf, lane * 16, (t16a * NUK + i * 2 + kp) * 1024);
    f32x4 accb[4];
#pragma unroll
    for (int n = 0; n < 4; ++n) accb[n] = (f32x4){0.f, 0.f, 0.f, 0.f};
#pragma unroll
    for (int i = 0; i < 11; ++i) {
#pragma unroll
      for (int n = 0; n < 4; ++n) { PIN(wl[i][n]); PIN(wh[i][n]); accb[n] = mfma32(cat8(wl[i][n], wh[i][n]), __builtin_bit_cast(bf16x8, aq[i]), accb[n]); }
    }
#pragma unroll
    for (int n = 0; n < 4; ++n)
      *(LAS f32x4*)(smem + kp * 17408 + (mw * 16 + fr) * 272 + (n * 16 + fq * 4) * 4) = acca[n] * gate + accb[n] * (1.0f - gate);
    WAIT_L(0); BAR();
    const int r = tid >> 3, ch = tid & 7;
    lds_char* p = smem + r * 272 + ch * 32;
    f32x4 a0 = *(LAS f32x4*)p + *(LAS f32x4*)(p + 17408);
    f32x4 a1 = *(LAS f32x4*)(p + 16) + *(LAS f32x4*)(p + 17408 + 16);
    u32x4 w;
    w.x = pk_bf16(a0[0], a0[1]); w.y = pk_bf16(a0[2], a0[3]); w.z = pk_bf16(a1[0], a1[1]); w.w = pk_bf16(a1[2], a1[3]);
    *(u32x4*)((char*)out + (size_t)(tt * 64 + r) * 1024 + nj * 128 + ch * 16) = w;
  }
  STAMP(15);
  asm volatile("gm_code_b:");
}

extern "C" void kernel_launch(void* const* d_in, const int* in_sizes, int n_in, void* d_out, int out_size, void* d_ws,
                              size_t ws_size, hipStream_t stream) {
  (void)hipFuncSetAttribute((const void*)gm_fused, hipFuncAttributeMaxDynamicSharedMemorySize, LDS_BYTES);
  gm_fused<<<256, 512, LDS_BYTES, stream>>>((const __hip_bfloat16*)d_in[0], (const __hip_bfloat16*)d_in[1], (const __hip_bfloat16*)d_in[2],
                                            (const __hip_bfloat16*)d_in[4], (const __hip_bfloat16*)d_in[5], (const __hip_bfloat16*)d_in[6],
                                            (const __hip_bfloat16*)d_in[7], (const __hip_bfloat16*)d_in[8], (char*)d_ws, (__hip_bfloat16*)d_out);
}
```

```cpp
#include <hip/hip_runtime.h>
#include <hip/hip_bf16.h>
#include <stdint.h>
#include <type_traits>

typedef short bf16x8 __attribute__((ext_vector_type(8)));
typedef short bf16x4 __attribute__((ext_vector_type(4)));
typedef float f32x4 __attribute__((ext_vector_type(4)));
typedef float f32x2 __attribute__((ext_vector_type(2)));
typedef unsigned u32x4 __attribute__((ext_vector_type(4)));
typedef unsigned u32x2 __attribute__((ext_vector_type(2)));
typedef unsigned long long u64;
#define LAS __attribute__((address_space(3)))
#define GAS __attribute__((address_space(1)))
typedef LAS unsigned lds_u32;
typedef LAS char lds_char;
typedef __amdgpu_buffer_rsrc_t rsrc_t;

constexpr int S_ = 2048, D_ = 512, F_ = 1376, FH = 688, NQKV = 1536;
constexpr int NFT = 43;
constexpr int NUK = 22;

constexpr size_t MB4 = 4u << 20;
constexpr size_t WS_OF = 0 * MB4 + 4096;
constexpr size_t WS_Z1 = 1 * MB4 + 4096;
constexpr size_t WS_Z2 = 2 * MB4 + 4096;
constexpr size_t WS_UF = 3 * MB4 + 4096;
constexpr size_t WS_ST = 4 * MB4 + 4096;
constexpr size_t WS_GT = WS_ST + (size_t)2048 * 8 * 16;
constexpr size_t WS_FO = WS_GT + 2048 * 4;
constexpr size_t WS_F2 = WS_FO + 2048;
constexpr size_t WS_FU = WS_F2 + 16384;
constexpr size_t WS_EP = WS_FU + 2048;
constexpr size_t WS_END = WS_EP + 256;

constexpr int SLOT = 8192;
constexpr int LDS_BYTES = 20 * SLOT;
constexpr int AUX_SC1 = 16;

#define WAIT_V(n) asm volatile("s_waitcnt vmcnt(%0)" ::"n"(n) : "memory")
#define WAIT_L(n) asm volatile("s_waitcnt lgkmcnt(%0)" ::"n"(n) : "memory")
#define FENCE() asm volatile("" ::: "memory")
#define BAR() do { asm volatile("" ::: "memory"); __builtin_amdgcn_s_barrier(); asm volatile("" ::: "memory"); } while (0)
#define SCHED() __builtin_amdgcn_sched_barrier(0)
#define PIN(x) asm volatile("" : "+v"(x))
#define RLX_AGENT __ATOMIC_RELAXED, __HIP_MEMORY_SCOPE_AGENT
#define P2IT(it) (((it) & ~3) + (((it) + rot2) & 3))

__device__ __forceinline__ rsrc_t mk_rsrc(const void* p, unsigned bytes) {
  return __builtin_amdgcn_make_buffer_rsrc((void*)p, 0, bytes, 0x00020000);
}
template <int AUX = 0>
__device__ __forceinline__ void glds16(rsrc_t r, lds_char* dst, unsigned voff, unsigned soff) {
  __builtin_amdgcn_raw_ptr_buffer_load_lds(r, (lds_u32*)dst, 16, voff, soff, 0, AUX);
}
__device__ __forceinline__ void st16_sc1(rsrc_t r, unsigned voff, u32x4 v) { __builtin_amdgcn_raw_buffer_store_b128(v, r, voff, 0, AUX_SC1); }
__device__ __forceinline__ u32x4 ld16_sc1(rsrc_t r, unsigned voff, unsigned soff) { return __builtin_amdgcn_raw_buffer_load_b128(r, voff, soff, AUX_SC1); }
__device__ __forceinline__ unsigned pk_bf16(float lo, float hi) {
  typedef __bf16 bf2 __attribute__((ext_vector_type(2)));
  f32x2 v = {lo, hi};
  return __builtin_bit_cast(unsigned, __builtin_convertvector(v, bf2));
}
__device__ __forceinline__ float fma1(float a, float b, float c) { float d; asm("v_fma_f32 %0, %1, %2, %3" : "=v"(d) : "v"(a), "v"(b), "v"(c)); return d; }
__device__ __forceinline__ float bf_lo(unsigned w) { return __uint_as_float(w << 16); }
__device__ __forceinline__ float bf_hi(unsigned w) { return __uint_as_float(w & 0xffff0000u); }
template <int OFF, class T>
__device__ __forceinline__ void rd128(T& d, unsigned addr) {
  asm volatile("ds_read_b128 %0, %1 offset:%2" : "=v"(d) : "v"(addr), "n"(OFF) : "memory");
}
template <int OFF>
__device__ __forceinline__ void tr_frag(bf16x4& lo, bf16x4& hi, unsigned addr) {
  asm volatile("ds_read_b64_tr_b16 %0, %2 offset:%3\n\tds_read_b64_tr_b16 %1, %2 offset:%4" : "=&v"(lo), "=&v"(hi) : "v"(addr), "n"(OFF), "n"(OFF + 256) : "memory");
}
__device__ __forceinline__ bf16x8 cat8(bf16x4 a, bf16x4 b) { return __builtin_shufflevector(a, b, 0, 1, 2, 3, 4, 5, 6, 7); }
__device__ __forceinline__ f32x4 mfma32(bf16x8 a, bf16x8 b, f32x4 c) { return __builtin_amdgcn_mfma_f32_16x16x32_bf16(a, b, c, 0, 0, 0); }
__device__ __forceinline__ f32x4 mfma16(bf16x4 a, bf16x4 b, f32x4 c) { return __builtin_amdgcn_mfma_f32_16x16x16bf16_1k(a, b, c, 0, 0, 0); }

__device__ __forceinline__ float rows_max(float v) {
  auto a = __builtin_amdgcn_permlane16_swap(__float_as_uint(v), __float_as_uint(v), false, false);
  v = fmaxf(__uint_as_float(a[0]), __uint_as_float(a[1]));
  auto b = __builtin_amdgcn_permlane32_swap(__float_as_uint(v), __float_as_uint(v), false, false);
  return fmaxf(__uint_as_float(b[0]), __uint_as_float(b[1]));
}
__device__ __forceinline__ float rows_sum(float v) {
  auto a = __builtin_amdgcn_permlane16_swap(__float_as_uint(v), __float_as_uint(v), false, false);
  v = __uint_as_float(a[0]) + __uint_as_float(a[1]);
  auto b = __builtin_amdgcn_permlane32_swap(__float_as_uint(v), __float_as_uint(v), false, false);
  return __uint_as_float(b[0]) + __uint_as_float(b[1]);
}
__device__ __forceinline__ float row16_sum(float v) {
  v += __int_as_float(__builtin_amdgcn_update_dpp(0, __float_as_int(v), 0xB1, 0xf, 0xf, false));
  v += __int_as_float(__builtin_amdgcn_update_dpp(0, __float_as_int(v), 0x4E, 0xf, 0xf, false));
  v += __int_as_float(__builtin_amdgcn_update_dpp(0, __float_as_int(v), 0x141, 0xf, 0xf, false));
  v += __int_as_float(__builtin_amdgcn_update_dpp(0, __float_as_int(v), 0x140, 0xf, 0xf, false));
  return v;
}
__device__ __forceinline__ float gelu_tanh(float x) {
  const float c1 = -2.0f * 0.7978845608028654f * 1.4426950408889634f;
  const float c3 = c1 * 0.044715f;
  float t = x * (c1 + c3 * (x * x));
  return x * __builtin_amdgcn_rcpf(1.0f + __builtin_amdgcn_exp2f(t));
}
__device__ __forceinline__ f32x4 gelu4(f32x4 v) { return (f32x4){gelu_tanh(v[0]), gelu_tanh(v[1]), gelu_tanh(v[2]), gelu_tanh(v[3])}; }
__device__ __forceinline__ u32x4 swap_pair(u32x2 a, u32x2 b) {
  auto x = __builtin_amdgcn_permlane16_swap(a.x, b.x, false, false);
  auto y = __builtin_amdgcn_permlane16_swap(a.y, b.y, false, false);
  u32x4 r; r.x = x[0]; r.y = y[0]; r.z = x[1]; r.w = y[1];
  return r;
}
__device__ __forceinline__ u32x2 pk4(f32x4 v) { u32x2 r; r.x = pk_bf16(v[0], v[1]); r.y = pk_bf16(v[2], v[3]); return r; }
__device__ __forceinline__ u64 ldflag(const char* ws, unsigned off) { return __hip_atomic_load((GAS u64*)(ws + off), RLX_AGENT); }
#define PSLEEP 1
__device__ __forceinline__ void poll_flags(const char* ws, unsigned off, u64 tag) {
#pragma unroll 1
  for (unsigned spins = 0; spins < (1u << 20); ++spins) {
    if (__all(ldflag(ws, off) == tag)) break;
    __builtin_amdgcn_s_sleep(PSLEEP);
  }
}
template <int N> using IC = std::integral_constant<int, N>;
template <int N, class F> __device__ __forceinline__ void sfor(F f) {
  if constexpr (N > 0) { sfor<N - 1>(f); f(IC<N - 1>{}); }
}

#define NRM 2
constexpr int QBUF = 47104;
constexpr int Q_W = 10240, Q_Z = 34816;
constexpr int ZRING = 2 * QBUF, ZBUF = 20480;
constexpr int SCR = 18 * SLOT;
#define ROTW 3

__global__ __launch_bounds__(512, 2) void gm_fused(const __hip_bfloat16* x, const __hip_bfloat16* wqkv, const __hip_bfloat16* wo,
                                                   const __hip_bfloat16* win, const __hip_bfloat16* wsp, const __hip_bfloat16* bsp,
                                                   const __hip_bfloat16* wout, const __hip_bfloat16* gw, char* ws, __hip_bfloat16* out) {
  extern __shared__ __attribute__((aligned(16))) char smem_[];
  lds_char* smem = (lds_char*)smem_;
  const unsigned lds = (unsigned)(size_t)smem;
  const int tid = threadIdx.x;
  const int wid = __builtin_amdgcn_readfirstlane(tid >> 6), lane = tid & 63, fr = lane & 15, fq = lane >> 4;
  const int xcd = blockIdx.x & 7, idx = blockIdx.x >> 3;
  const int tt = (xcd >> 1) * 8 + (idx & 7), fj = (xcd & 1) * 4 + (idx >> 3), nj = fj;
  const int rot2 = (idx + (idx >> 3)) & 3;
  const int g = fq, q4 = (lane & 15) >> 2, p4 = lane & 3;
  const unsigned tr_e = q4 * 64 + (2 * (g & 1) + (p4 >> 1)) * 16 + (p4 & 1) * 8;
  const unsigned tr_o = q4 * 64 + (2 * (1 - (g & 1)) + (p4 >> 1)) * 16 + (p4 & 1) * 8;
  const unsigned sw8 = ((lane & 7) ^ (lane >> 3)) * 16;
  const int ftb = fj < 3 ? fj * 6 : 18 + (fj - 3) * 5;
  const int nft = fj < 3 ? 6 : 5;
  constexpr int pitchW = F_ * 2;
  rsrc_t rx = mk_rsrc(x, S_ * D_ * 2);
  rsrc_t rwz = mk_rsrc(win, D_ * pitchW);
  rsrc_t rwq = mk_rsrc(wqkv, D_ * NQKV * 2);
  auto KT = [&](int u) { return (u & ~ROTW) + ((u + idx + (idx >> 3)) & ROTW); };
  auto z_off = [&](int p) -> unsigned {
    const int st = 2 * p + (lane >> 5), kb = st / 3, cb = st - kb * 3;
    int lc = cb * 32 + (((lane & 3) ^ (2 * (kb & 1))) * 8);
    if (lc >= nft * 16) lc -= 16;
    return (unsigned)((kb * 8 + ((lane & 31) >> 2)) * pitchW + (ftb * 16 + lc) * 2);
  };
  const unsigned zx = (unsigned)((tt * 64 + wid * 8 + (lane >> 3)) * 1024) + sw8;
  const unsigned zw0 = z_off(wid), zw1 = z_off(8 + (wid & 3));
  auto LDZ = [&](int i, int v) {
    const int kt = KT(v);
    lds_char* b = smem + ZRING + (v % 3) * ZBUF + wid * 1024;
    if (i == 0) glds16(rx, b, zx, kt * 128);
    if (i == 1) glds16(rwz, b + 8192, zw0, kt * (64 * pitchW) + FH * 2);
    if (i == 2 && wid < 4) glds16(rwz, b + 16384, zw1, kt * (64 * pitchW) + FH * 2);
  };
#pragma unroll
  for (int v = 0; v < 3; ++v) {
#pragma unroll
    for (int i = 0; i < 3; ++i) LDZ(i, v);
    if (v == 0) BAR();
    FENCE();
  }
  u64 tag = ldflag(ws, WS_EP);
  FENCE();
  const int lw = wid & 3;
  auto qx_off = [&](int p) -> unsigned { int gr = tt * 64 - 16 + 8 * p + (lane >> 3); gr = gr < 0 ? 0 : gr; return (unsigned)(gr * 1024) + sw8; };
  const unsigned qx0 = qx_off(lw), qx1 = qx_off(lw + 4), qx2 = qx_off(lw < 2 ? lw + 8 : lw + 4);
  const unsigned qw0 = (unsigned)((lw * 16 + ((lane & 31) >> 2)) * (NQKV * 2) + (fj * 64 + (lane >> 5) * 32 + (lane & 3) * 8) * 2);
  const unsigned qw1 = (unsigned)((lw * 16 + 8 + ((lane & 31) >> 2)) * (NQKV * 2) + (fj * 64 + (lane >> 5) * 32 + ((lane & 3) ^ 2) * 8) * 2);
  const unsigned qz0 = z_off(lw * 3), qz1 = z_off(lw * 3 + 1), qz2 = z_off(lw * 3 + 2);
  const unsigned x2dst = (lw < 2 ? lw + 8 : lw + 4) * 1024;
  auto LDQ = [&](int i, int u) {
    const int kt = KT(u);
    lds_char* b = smem + (u % 3) * QBUF;
    const unsigned sw = kt * (64 * NQKV * 2), sz = kt * (64 * pitchW);
    if (i == 0) glds16(rx, b + lw * 1024, qx0, kt * 128);
    if (i == 1) glds16(rwq, b + Q_W + lw * 6144, qw0, sw);
    if (i == 2) glds16(rx, b + (lw + 4) * 1024, qx1, kt * 128);
    if (i == 3) glds16(rwq, b + Q_W + lw * 6144 + 3072, qw1, sw);
    if (i == 4) glds16(rx, b + x2dst, qx2, kt * 128);
    if (i == 5) glds16(rwq, b + Q_W + lw * 6144 + 1024, qw0, sw + 1024);
    if (i == 6) glds16(rwq, b + Q_W + lw * 6144 + 4096, qw1, sw + 1024);
    if (i == 7) glds16(rwz, b + Q_Z + lw * 3072, qz0, sz);
    if (i == 8) glds16(rwq, b + Q_W + lw * 6144 + 2048, qw0, sw + 2048);
    if (i == 9) glds16(rwq, b + Q_W + lw * 6144 + 5120, qw1, sw + 2048);
    if (i == 10) glds16(rwz, b + Q_Z + lw * 3072 + 1024, qz1, sz);
    if (i == 11) glds16(rwz, b + Q_Z + lw * 3072 + 2048, qz2, sz);
  };
  rsrc_t rsp = mk_rsrc(wsp, S_ * S_ * 2);
  const unsigned sp_off = (unsigned)((tt * 64 + wid * 8 + (lane >> 3)) * 4096) + sw8;
  const unsigned sp_l = (unsigned)((tt * 64 + lw * 16 + (lane >> 3)) * 4096) + sw8;
  auto pf_l = [&](int s, int h) { glds16(rsp, smem + s * SLOT + (lw * 2 + h) * 1024, sp_l, P2IT(s >> 1) * 256 + (s & 1) * 128 + h * 32768); };

  const int zm = wid & 3, nh = wid >> 2;
  const int znv = nft - nh * 3;
  f32x4 g2[3];
  {
    const unsigned a0 = lds + ZRING + (zm * 16 + fr) * 128 + ((fq ^ (fr & 7)) * 16), a1 = a0 ^ 64u;
    unsigned b_rd[3];
#pragma unroll
    for (int n = 0; n < 3; ++n) { const int t = nh * 3 + n; b_rd[n] = lds + ZRING + 8192 + g * 1536 + ((t & 1) ? tr_o : tr_e) + (t >> 1) * 512; }
#pragma unroll
    for (int n = 0; n < 3; ++n) g2[n] = (f32x4){0.f, 0.f, 0.f, 0.f};
    struct Set { bf16x8 a[2]; bf16x4 l[2][3], h[2][3]; };
    Set X, Y;
    auto rdg = [&](Set& s, auto V, auto J) {
      constexpr int v = decltype(V)::value, j = decltype(J)::value, ks = j >> 2, i = j & 3, bo = (v % 3) * ZBUF;
      if constexpr (i == 0) rd128<bo>(s.a[ks], ks ? a1 : a0);
      else tr_frag<bo + ks * 6144>(s.l[ks][i - 1], s.h[ks][i - 1], b_rd[i - 1]);
    };
    WAIT_V(5); BAR();
    sfor<8>([&](auto J) { rdg(X, IC<0>{}, J); });
    sfor<8>([&](auto V) {
      constexpr int v = decltype(V)::value;
      Set& c = (v & 1) ? Y : X; Set& n = (v & 1) ? X : Y;
      WAIT_L(0);
      if constexpr (v < 7) WAIT_V(v < 6 ? 2 : 6);
      BAR();
#pragma unroll
      for (int ks = 0; ks < 2; ++ks) {
        PIN(c.a[ks]);
#pragma unroll
        for (int i = 0; i < 3; ++i) { PIN(c.l[ks][i]); PIN(c.h[ks][i]); }
      }
      sfor<6>([&](auto J) {
        constexpr int j = decltype(J)::value, ks = j / 3, nn = j % 3;
        g2[nn] = mfma32(c.a[ks], cat8(c.l[ks][nn], c.h[ks][nn]), g2[nn]);
        SCHED();
        if constexpr (v < 5) { if constexpr (j < 2) LDZ(j, v + 3); }
        else if constexpr (v < 7) LDQ((v - 5) * 6 + j, nh);
        else { if (nh == 0) LDQ(j, 2); else LDQ(6 + j, 2); }
        if constexpr (v < 7) { rdg(n, IC<v + 1>{}, J); if constexpr (j >= 4) rdg(n, IC<v + 1>{}, IC<j + 2>{}); }
        SCHED();
      });
      if constexpr (v < 5) LDZ(2, v + 3);
    });
  }
  PIN(tag); tag += 1ull;
  const unsigned tlo = (unsigned)tag, thi = (unsigned)(tag >> 32);
  rsrc_t rst = mk_rsrc(ws + WS_ST, 2048 * 128);
  {
#pragma unroll
    for (int n = 0; n < 3; ++n) g2[n] = gelu4(g2[n]);
    const f32x4 z4 = {0.f, 0.f, 0.f, 0.f};
    const f32x4 a2 = znv > 2 ? g2[2] : z4;
    const float w2 = znv > 2 ? 1.f : 0.f, icnt = znv > 2 ? (1.f / 48.f) : (1.f / 32.f);
    f32x4 sm = g2[0] + g2[1] + a2, mean, m2;
#pragma unroll
    for (int r = 0; r < 4; ++r) mean[r] = row16_sum(sm[r]) * icnt;
    const f32x4 d0 = g2[0] - mean, d1 = g2[1] - mean, d2 = (a2 - mean) * w2;
    sm = d0 * d0 + d1 * d1 + d2 * d2;
#pragma unroll
    for (int r = 0; r < 4; ++r) m2[r] = row16_sum(sm[r]);
    const float mv = fr == 0 ? mean[0] : (fr == 1 ? mean[1] : (fr == 2 ? mean[2] : mean[3]));
    const float qv = fr == 0 ? m2[0] : (fr == 1 ? m2[1] : (fr == 2 ? m2[2] : m2[3]));
    if (fr < 4) asm volatile("ds_write_b64 %0, %1" :: "v"(lds + SCR + 12288 + ((zm * 16 + 4 * fq + fr) * 2 + nh) * 8), "v"((f32x2){mv, qv}) : "memory");
  }
  rsrc_t rz2 = mk_rsrc(ws + WS_Z2, NFT * 64 * 1024);
  const unsigned goff = (unsigned)((tt * 64 + zm * 16 + (lane >> 2)) * 128 + (lane & 3) * 32);
  u32x4 pt[2];
  auto gather = [&]() { pt[0] = ld16_sc1(rst, goff, 0); pt[1] = ld16_sc1(rst, goff, 16); };
  auto gather_ok = [&]() { return (bool)__all(pt[0].y == tlo && pt[0].w == thi && pt[1].y == tlo && pt[1].w == thi); };
  auto rowstats = [&]() {
    if (!gather_ok()) {
#pragma unroll 1
      for (unsigned spins = 0; spins < (1u << 20); ++spins) { gather(); if (gather_ok()) break; __builtin_amdgcn_s_sleep(4); }
    }
    const float n0 = (lane & 3) >= 2 ? 80.f : 96.f, n1 = (lane & 3) >= 1 ? 80.f : 96.f;
    const float a0 = __uint_as_float(pt[0].x), a1 = __uint_as_float(pt[1].x);
    float sm = n0 * a0 + n1 * a1;
    sm += __int_as_float(__builtin_amdgcn_update_dpp(0, __float_as_int(sm), 0xB1, 0xf, 0xf, false));
    sm += __int_as_float(__builtin_amdgcn_update_dpp(0, __float_as_int(sm), 0x4E, 0xf, 0xf, false));
    const float mu = sm * (1.0f / 688.f), d0 = a0 - mu, d1 = a1 - mu;
    float q = __uint_as_float(pt[0].z) + __uint_as_float(pt[1].z) + n0 * d0 * d0 + n1 * d1 * d1;
    q += __int_as_float(__builtin_amdgcn_update_dpp(0, __float_as_int(q), 0xB1, 0xf, 0xf, false));
    q += __int_as_float(__builtin_amdgcn_update_dpp(0, __float_as_int(q), 0x4E, 0xf, 0xf, false));
    const float rstd = 1.0f / sqrtf(q * (1.0f / 688.f) + 1e-5f);
    asm volatile("ds_write_b32 %0, %1" :: "v"(lds + SCR + 13312 + zm * 128 + ((lane & 1) * 16 + (lane >> 2)) * 4), "v"((lane & 1) ? -mu * rstd : rstd) : "memory");
  };
  auto normalise = [&]() {
    f32x4 r0, c0;
    asm volatile("ds_read_b128 %0, %2\n\tds_read_b128 %1, %2 offset:64\n\ts_waitcnt lgkmcnt(0)" : "=&v"(r0), "=&v"(c0) : "v"(lds + SCR + 13312 + zm * 128 + fq * 16) : "memory");
    const u32x2 y0 = pk4(g2[0] * r0 + c0), y1 = pk4(g2[1] * r0 + c0), y2 = pk4(g2[2] * r0 + c0);
    const unsigned zo = (unsigned)(((ftb + nh * 3) * 64 + tt * 2 + (zm >> 1)) * 1024 + ((2 * (zm & 1) + (fq >> 1)) * 16 + fr) * 16);
    st16_sc1(rz2, zo + (fq & 1) * 65536u, swap_pair(y0, y1));
    const u32x4 w = swap_pair(y2, y2);
    if (znv > 2 && !(fq & 1)) st16_sc1(rz2, zo + 2 * 65536u, w);
  };
  const unsigned f2_off = (unsigned)(WS_F2 + xcd * 2048 + (fj * 32 + (lane & 31)) * 8);
  u64 fz_a;
  constexpr int LB = 6;
  const int fh = wid & 1, kg = wid >> 1;
  const int ft0 = ftb + fh * 3;
  const bool has2 = fh * 3 + 2 < nft;
  const unsigned zb_off = (unsigned)((ft0 * 64 + kg) * 1024 + lane * 16);
  const unsigned zb_n2 = has2 ? 2 * 65536u : 65536u;
  u32x4 bq[LB][3];
  auto issueB = [&](int it) {
    const unsigned so = (unsigned)(P2IT(it) * 4096);
    bq[it % LB][0] = ld16_sc1(rz2, zb_off, so); bq[it % LB][1] = ld16_sc1(rz2, zb_off, so + 65536u); bq[it % LB][2] = ld16_sc1(rz2, zb_off, so + zb_n2);
  };

  WAIT_V(6); WAIT_L(0); BAR();
  if (wid == 7) {
    f32x4 p;
    asm volatile("ds_read_b128 %0, %1\n\ts_waitcnt lgkmcnt(0)" : "=&v"(p) : "v"(lds + SCR + 12288 + lane * 16) : "memory");
    const float nb = (float)(nft * 16 - 48), nt = 48.f + nb, d = p[2] - p[0];
    const float mu = p[0] + d * (nb / nt), q = p[1] + p[3] + d * d * (48.f * nb / nt);
    st16_sc1(rst, (unsigned)((tt * 64 + lane) * 128 + fj * 16), (u32x4){__float_as_uint(mu), tlo, __float_as_uint(q), thi});
  }
  if (wid < 4) {
    const unsigned a_rd0 = lds + fr * 128 + ((fq ^ (fr & 7)) * 16), a_rd1 = a_rd0 ^ 64u;
    const unsigned b_rd = lds + Q_W + g * 3072 + ((lw & 1) ? tr_o : tr_e) + (lw >> 1) * 512;
    f32x4 acc[14];
#pragma unroll
    for (int i = 0; i < 14; ++i) acc[i] = (f32x4){0.f, 0.f, 0.f, 0.f};
    struct Set { bf16x8 a[2][5]; bf16x4 l[2][3], h[2][3]; };
    Set X, Y;
    auto rdg = [&](Set& s, auto U, auto J) {
      constexpr int u = decltype(U)::value, j = decltype(J)::value, ks = j >> 3, i = j & 7;
      constexpr int bo = (u % 3 == 1) ? QBUF : 0;
      const unsigned aa = (ks ? a_rd1 : a_rd0) + (u % 3 == 2 ? 2u * QBUF : 0u), bb = b_rd + (u % 3) * (unsigned)QBUF;
      if constexpr (i == 0) tr_frag<ks * 12288>(s.l[ks][0], s.h[ks][0], bb);
      else if constexpr (i <= 4) rd128<bo + i * 2048>(s.a[ks][i], aa);
      else if constexpr (i == 5) tr_frag<ks * 12288 + 1024>(s.l[ks][1], s.h[ks][1], bb);
      else if constexpr (i == 6) rd128<bo>(s.a[ks][0], aa);
      else tr_frag<ks * 12288 + 2048>(s.l[ks][2], s.h[ks][2], bb);
    };
    sfor<16>([&](auto J) { rdg(X, IC<0>{}, J); });
    sfor<8>([&](auto U) {
      constexpr int u = decltype(U)::value;
      Set& c = (u & 1) ? Y : X; Set& n = (u & 1) ? X : Y;
      WAIT_L(0);
      if constexpr (u >= 1 && u < 7) WAIT_V(6);
      BAR();
      if constexpr (u == NRM + 3) { if (tid < 8) __hip_atomic_store((GAS u64*)(ws + WS_F2) + tid * 256 + fj * 32 + tt, tag, RLX_AGENT); }
#pragma unroll
      for (int ks = 0; ks < 2; ++ks) {
#pragma unroll
        for (int i = 0; i < 5; ++i) PIN(c.a[ks][i]);
#pragma unroll
        for (int i = 0; i < 3; ++i) { PIN(c.l[ks][i]); PIN(c.h[ks][i]); }
      }
      if constexpr (u == 7) { fz_a = ldflag(ws, f2_off); SCHED(); }
      sfor<28>([&](auto J) {
        constexpr int j = decltype(J)::value, ks = j / 14, i = j % 14;
        if constexpr (i < 4) acc[i] = mfma32(cat8(c.l[ks][0], c.h[ks][0]), c.a[ks][i + 1], acc[i]);
        else if constexpr (i < 9) acc[i] = mfma32(cat8(c.l[ks][1], c.h[ks][1]), c.a[ks][i - 4], acc[i]);
        else acc[i] = mfma32(c.a[ks][i - 9], cat8(c.l[ks][2], c.h[ks][2]), acc[i]);
        SCHED();
        if constexpr (j % 4 == 0 && j / 4 < 6) {
          if constexpr (u < 5) LDQ(j / 4, u + 3);
          else if constexpr (u == 5) pf_l(12 + j / 4, 0);
          else if constexpr (u == 6) { if constexpr (j / 4 < 5) pf_l(j / 4, 0); }
        }
        constexpr int r = j - (j + 3) / 4;
        if constexpr (u < 7 && j % 4 != 0 && r < 16) rdg(n, IC<u + 1>{}, IC<r>{});
        SCHED();
      });
      if constexpr (u == NRM + 1) { normalise(); }
    });
    bf16x4 qb[4], kb[5], vb[5];
#pragma unroll
    for (int i = 0; i < 4; ++i) qb[i] = __builtin_bit_cast(bf16x4, pk4(acc[i]));
#pragma unroll
    for (int i = 0; i < 5; ++i) { kb[i] = __builtin_bit_cast(bf16x4, pk4(acc[4 + i])); vb[i] = __builtin_bit_cast(bf16x4, pk4(acc[9 + i])); }
    const int par = fr & 1;
    const int j0 = 4 * fq + par, j1 = j0 + 2;
    const bool v00 = j0 >= fr + 2, v01 = j1 >= fr + 2, v10 = j0 <= fr, v11 = j1 <= fr;
    const float sc = 0.25f * 1.4426950408889634f, NEG = -3.0e38f;
    u32x2 ov[4];
#pragma unroll
    for (int i = 0; i < 4; ++i) {
      f32x4 z4 = {0.f, 0.f, 0.f, 0.f};
      f32x4 s0 = mfma16(kb[i], qb[i], z4);
      f32x4 s1 = mfma16(kb[i + 1], qb[i], z4);
      float a0 = par ? s0[1] : s0[0], a1 = par ? s0[3] : s0[2];
      float b0 = par ? s1[1] : s1[0], b1 = par ? s1[3] : s1[2];
      const bool prev_ok = !(i == 0 && tt == 0);
      const bool m00 = v00 && prev_ok, m01 = v01 && prev_ok;
      a0 = m00 ? a0 : NEG; a1 = m01 ? a1 : NEG; b0 = v10 ? b0 : NEG; b1 = v11 ? b1 : NEG;
      float mx = rows_max(fmaxf(fmaxf(a0, a1), fmaxf(b0, b1)));
      float e00 = m00 ? __builtin_amdgcn_exp2f((a0 - mx) * sc) : 0.f;
      float e01 = m01 ? __builtin_amdgcn_exp2f((a1 - mx) * sc) : 0.f;
      float e10 = v10 ? __builtin_amdgcn_exp2f((b0 - mx) * sc) : 0.f;
      float e11 = v11 ? __builtin_amdgcn_exp2f((b1 - mx) * sc) : 0.f;
      float inv = __builtin_amdgcn_rcpf(rows_sum((e00 + e01) + (e10 + e11)));
      u32x4 pw;
      const unsigned sh = par * 16;
      pw.x = (pk_bf16(e00 * inv, 0.f) & 0xffffu) << sh;
      pw.y = (pk_bf16(e01 * inv, 0.f) & 0xffffu) << sh;
      pw.z = (pk_bf16(e10 * inv, 0.f) & 0xffffu) << sh;
      pw.w = (pk_bf16(e11 * inv, 0.f) & 0xffffu) << sh;
      ov[i] = pk4(mfma32(cat8(vb[i], vb[i + 1]), __builtin_bit_cast(bf16x8, pw), z4));
    }
    const int h = fj * 4 + lw;
    rsrc_t ro = mk_rsrc(ws + WS_OF, 128 * 16 * 1024);
    const unsigned oo = (unsigned)(((tt * 4 + (fq & 1)) * 16 + (h >> 1)) * 1024 + ((2 * (h & 1) + (fq >> 1)) * 16 + fr) * 16);
    st16_sc1(ro, oo, swap_pair(ov[0], ov[1])); st16_sc1(ro, oo + 2 * 16384, swap_pair(ov[2], ov[3]));
  } else {
    const int mp = wid & 1, zh = (wid >> 1) & 1;
    const unsigned a_rd0 = lds + (16 + mp * 32 + fr) * 128 + ((fq ^ (fr & 7)) * 16), a_rd1 = a_rd0 ^ 64u;
    unsigned b_rd[3];
#pragma unroll
    for (int n = 0; n < 3; ++n) { const int t = zh * 3 + n; b_rd[n] = lds + Q_Z + g * 1536 + ((t & 1) ? tr_o : tr_e) + (t >> 1) * 512; }
    f32x4 acc[2][3];
#pragma unroll
    for (int i = 0; i < 6; ++i) acc[i / 3][i % 3] = (f32x4){0.f, 0.f, 0.f, 0.f};
    struct Set { bf16x8 a[2][2]; bf16x4 l[2][3], h[2][3]; };
    Set X, Y;
    auto rdg = [&](Set& s, auto U, auto J) {
      constexpr int u = decltype(U)::value, j = decltype(J)::value, ks = j / 5, i = j % 5;
      constexpr int bo = (u % 3 == 1) ? QBUF : 0;
      const unsigned hi = (u % 3 == 2 ? 2u * QBUF : 0u);
      if constexpr (i == 0) rd128<bo>(s.a[ks][0], (ks ? a_rd1 : a_rd0) + hi);
      else if constexpr (i == 2) rd128<bo + 2048>(s.a[ks][1], (ks ? a_rd1 : a_rd0) + hi);
      else { constexpr int n = i == 1 ? 0 : i - 2; tr_frag<bo + ks * 6144>(s.l[ks][n], s.h[ks][n], b_rd[n] + hi); }
    };
    const int grow = tt * 64 + fj * 8 + lw * 2;
    u32x4 gx0, gx1, gv;
    sfor<10>([&](auto J) { rdg(X, IC<0>{}, J); });
    sfor<8>([&](auto U) {
      constexpr int u = decltype(U)::value;
      Set& c = (u & 1) ? Y : X; Set& n = (u & 1) ? X : Y;
      WAIT_L(0);
      if constexpr (u >= 1 && u < 7) WAIT_V(6);
      BAR();
#pragma unroll
      for (int ks = 0; ks < 2; ++ks) {
        PIN(c.a[ks][0]); PIN(c.a[ks][1]);
#pragma unroll
        for (int i = 0; i < 3; ++i) { PIN(c.l[ks][i]); PIN(c.h[ks][i]); }
      }
      if constexpr (u == NRM) { gather(); SCHED(); }
      if constexpr (u == 7) {
        fz_a = ldflag(ws, f2_off);
        gx0 = *(const u32x4*)((const char*)x + (size_t)grow * 1024 + lane * 16);
        gx1 = *(const u32x4*)((const char*)x + (size_t)grow * 1024 + 1024 + lane * 16);
        gv = *(const u32x4*)((const char*)gw + lane * 16);
        SCHED();
      }
      sfor<12>([&](auto J) {
        constexpr int j = decltype(J)::value, ks = j / 6, nn = (j % 6) >> 1, m = j & 1;
        acc[m][nn] = mfma32(cat8(c.l[ks][nn], c.h[ks][nn]), c.a[ks][m], acc[m][nn]);
        SCHED();
        if constexpr (u < 7 && j < 10) rdg(n, IC<u + 1>{}, J);
        if constexpr (j % 2 == 0) {
          if constexpr (u < 5) LDQ(6 + j / 2, u + 3);
          else if constexpr (u == 5) pf_l(12 + j / 2, 1);
          else if constexpr (u == 6) { if constexpr (j < 10) pf_l(j / 2, 1); }
        }
        SCHED();
      });
      if constexpr (u == NRM) rowstats();
      if constexpr (u == NRM + 1) { normalise(); }
    });
#pragma unroll
    for (int i = 0; i < 6; ++i)
      *(LAS u32x2*)(smem + SCR + ((mp * 2 + i / 3) * 6 + zh * 3 + i % 3) * 512 + lane * 8) = pk4(gelu4(acc[i / 3][i % 3]));
    float s0 = 0.f, s1 = 0.f;
#pragma unroll
    for (int i = 0; i < 4; ++i) {
      s0 += bf_lo(gx0[i]) * bf_lo(gv[i]) + bf_hi(gx0[i]) * bf_hi(gv[i]);
      s1 += bf_lo(gx1[i]) * bf_lo(gv[i]) + bf_hi(gx1[i]) * bf_hi(gv[i]);
    }
    s0 = rows_sum(row16_sum(s0)); s1 = rows_sum(row16_sum(s1));
    const float gt = __builtin_amdgcn_rcpf(1.0f + __builtin_amdgcn_exp2f(-(lane == 0 ? s0 : s1) * 1.4426950408889634f));
    if (lane < 2) __hip_atomic_store((GAS unsigned*)(ws + WS_GT) + grow + lane, __float_as_uint(gt), RLX_AGENT);
  }
  const bool early = __all(fz_a == tag);
  if (early) { issueB(0); issueB(1); }
  FENCE();
#pragma unroll
  for (int s = 5; s < 12; ++s) glds16(rsp, smem + s * SLOT + wid * 1024, sp_off, P2IT(s >> 1) * 256 + (s & 1) * 128);
  FENCE();
  WAIT_V(7); WAIT_L(0);
  if (wid == 0 && !early) poll_flags(ws, f2_off, tag);
  BAR();
  if (tid == 0) __hip_atomic_store((GAS u64*)(ws + WS_FO) + tt * 8 + fj, tag, RLX_AGENT);
  if (!early) { issueB(0); issueB(1); }
  FENCE();

  rsrc_t rwt = mk_rsrc(wout, FH * D_ * 2);
  const unsigned wt_off = (unsigned)((wid * 8 + ((lane & 31) >> 2)) * 1024 + (nj * 64 + (lane >> 5) * 32 + (((lane & 3) ^ (2 * (wid & 1))) * 8)) * 2);
  const int t16 = tt * 4 + kg;
  const int mw = wid & 3, kp = wid >> 2, t16a = tt * 4 + mw;
  f32x4 acc[4][3];
#pragma unroll
  for (int i = 0; i < 12; ++i) acc[i / 3][i % 3] = (f32x4){0.f, 0.f, 0.f, 0.f};
  const unsigned a_rd = lds + (kg >> 1) * SLOT + fr * 128 + (((4 * (kg & 1) + fq) ^ (fr & 7)) * 16);
  bf16x8 XA[4], YA[4];
  auto rd2 = [&](bf16x8 (&a)[4], auto IT, auto M) {
    constexpr int it = decltype(IT)::value, m = decltype(M)::value, off = (it % 9) * 2 * SLOT + m * 2048;
    if constexpr (off < 65536) rd128<off>(a[m], a_rd); else if constexpr (off < 131072) rd128<off - 65536>(a[m], a_rd + 65536u); else rd128<off - 131072>(a[m], a_rd + 131072u);
  };
  u64 f1v = 0; u32x2 z1v[3]; float bias = 0.f, gate = 0.f;
  u32x4 oq[8];
  rsrc_t rwo = mk_rsrc(wo, D_ * D_ * 2), rof = mk_rsrc(ws + WS_OF, 128 * 16 * 1024);
  const unsigned f1_off = (unsigned)(WS_FO + (tt * 8 + (lane & 7)) * 8);
  sfor<4>([&](auto M) { rd2(XA, IC<0>{}, M); });
  sfor<16>([&](auto IT) {
    constexpr int it = decltype(IT)::value;
    bf16x8 (&c)[4] = (it & 1) ? YA : XA; bf16x8 (&n)[4] = (it & 1) ? XA : YA;
    WAIT_L(0);
    if constexpr (it == 1) { WAIT_V(9); BAR(); }
    if constexpr (it == 8) WAIT_V(9);
    if constexpr (it == 12) WAIT_V(3);
    if constexpr (it == 4 || it == 8 || it == 12 || it == 14) BAR();
    SCHED();
#pragma unroll
    for (int i = 0; i < 4; ++i) PIN(c[i]);
    sfor<12>([&](auto J) {
      constexpr int j = decltype(J)::value, nn = j >> 2, m = j & 3;
      acc[m][nn] = mfma32(__builtin_bit_cast(bf16x8, bq[it % LB][nn]), c[m], acc[m][nn]);
      SCHED();
      if constexpr (it < 15 && j % 2 == 0 && j < 8) rd2(n, IC<it + 1>{}, IC<j / 2>{});
      if constexpr ((it == 4 || it == 5 || it == 6 || it == 8) && (j == 1 || j == 3 || j == 5 || j == 7)) {
        constexpr int sl = (it == 8 ? 10 : (it - 4) * 4) + (j >> 1);
        if constexpr (!(it == 6 && j > 3))
          glds16(rsp, smem + sl * SLOT + wid * 1024, sp_off, P2IT((sl >> 1) + 9) * 256 + (sl & 1) * 128);
      }
      if constexpr (it >= 12 && it < 14 && (j == 1 || j == 3 || j == 5 || j == 7)) {
        constexpr int kt = (it - 12) * 4 + (j >> 1);
        glds16(rwo, smem + kt * SLOT + wid * 1024, wt_off, kt * 65536);
      }
      if constexpr (it == 14 && (j == 1 || j == 3 || j == 5)) {
        constexpr int kt = j >> 1;
        glds16(rwt, smem + (8 + kt) * SLOT + wid * 1024, wt_off, kt * 65536);
      }
      if constexpr (it < 2 && j % 2 == 0)
        bq[2 + it * 2 + j / 6][(j / 2) % 3] = ld16_sc1(rz2, zb_off, (unsigned)(P2IT(2 + it * 2 + j / 6) * 4096) + ((j / 2) % 3 == 0 ? 0u : ((j / 2) % 3 == 1 ? 65536u : zb_n2)));
      if constexpr (it == 15 && j < 8) oq[j] = ld16_sc1(rof, lane * 16, (t16a * 16 + j * 2 + kp) * 1024);
      if constexpr (j % 4 == 3 && it + LB < 16)
        bq[it % LB][nn] = ld16_sc1(rz2, zb_off, (unsigned)(P2IT(it + LB) * 4096) + (nn == 0 ? 0u : (nn == 1 ? 65536u : zb_n2)));
      SCHED();
    });
    if constexpr (it == 8) f1v = ldflag(ws, f1_off);
    if constexpr (it == 12) {
      if (!__all(f1v == tag)) poll_flags(ws, f1_off, tag);
#pragma unroll
      for (int nn = 0; nn < 3; ++nn)
        asm volatile("ds_read_b64 %0, %1" : "=v"(z1v[nn]) : "v"(lds + SCR + (kg * 6 + fh * 3 + nn) * 512 + lane * 8) : "memory");
      bias = __bfloat162float(bsp[t16 * 16 + fr]);
      gate = __uint_as_float(__hip_atomic_load((GAS unsigned*)(ws + WS_GT) + t16a * 16 + fr, RLX_AGENT));
    }
    SCHED();
  });
  PIN(z1v[0]); PIN(z1v[1]); PIN(z1v[2]);
  BAR();
  lds_char* red = smem + 11 * SLOT;
  rsrc_t ruf = mk_rsrc(ws + WS_UF, 128 * NUK * 1024);
  {
#pragma unroll
    for (int m = 0; m < 4; ++m)
      if (m != kg) {
        const int src = kg - (kg > m ? 1 : 0);
#pragma unroll
        for (int n = 0; n < 3; ++n) *(LAS f32x4*)(red + (((fh * 4 + m) * 3 + src) * 3 + n) * 1024 + lane * 16) = acc[m][n];
      }
    WAIT_L(0); BAR();
    u32x2 uv[3];
#pragma unroll
    for (int n = 0; n < 3; ++n) {
      f32x4 gs = (kg == 0 ? acc[0][n] : (kg == 1 ? acc[1][n] : (kg == 2 ? acc[2][n] : acc[3][n]))) + bias;
#pragma unroll
      for (int s = 0; s < 3; ++s) gs += *(LAS f32x4*)(red + (((fh * 4 + kg) * 3 + s) * 3 + n) * 1024 + lane * 16);
      uv[n].x = pk_bf16(bf_lo(z1v[n].x) * gs[0], bf_hi(z1v[n].x) * gs[1]);
      uv[n].y = pk_bf16(bf_lo(z1v[n].y) * gs[2], bf_hi(z1v[n].y) * gs[3]);
    }
    if (!has2) uv[2] = (u32x2){0u, 0u};
    {
      const int ft = ft0 + (fq & 1);
      st16_sc1(ruf, (unsigned)((t16 * NUK + (ft >> 1)) * 1024 + ((2 * (ft & 1) + (fq >> 1)) * 16 + fr) * 16), swap_pair(uv[0], uv[1]));
    }
    if (has2 || ft0 + 2 == NFT) {
      const int ft = ft0 + 2;
      const u32x4 w = swap_pair(uv[2], uv[2]);
      if (!(fq & 1)) st16_sc1(ruf, (unsigned)((t16 * NUK + (ft >> 1)) * 1024 + ((2 * (ft & 1) + (fq >> 1)) * 16 + fr) * 16), w);
    }
  }
  WAIT_L(0); BAR();
  const unsigned w_rd_e = lds + (4 * kp + g) * 1024 + tr_e, w_rd_o = lds + (4 * kp + g) * 1024 + tr_o;
  f32x4 acca[4];
#pragma unroll
  for (int n = 0; n < 4; ++n) acca[n] = (f32x4){0.f, 0.f, 0.f, 0.f};
  {
#pragma unroll
    for (int k = 3; k < 11; ++k)
      glds16(rwt, smem + (8 + k) * SLOT + wid * 1024, wt_off - ((k == 10 && wid >= 6) ? 16384u : 0u), k * 65536);
    FENCE();
    WAIT_V(8); BAR();
    if (tid == 0) __hip_atomic_store((GAS u64*)(ws + WS_FU) + tt * 8 + fj, tag, RLX_AGENT);
    bf16x4 bl[8][4], bh[8][4];
#pragma unroll
    for (int i = 0; i < 8; ++i) {
      const unsigned so = i * SLOT;
      tr_frag<0>(bl[i][0], bh[i][0], w_rd_e + so); tr_frag<0>(bl[i][1], bh[i][1], w_rd_o + so);
      tr_frag<512>(bl[i][2], bh[i][2], w_rd_e + so); tr_frag<512>(bl[i][3], bh[i][3], w_rd_o + so);
      if (i >= 1) {
        WAIT_L(8);
#pragma unroll
        for (int n = 0; n < 4; ++n) { PIN(bl[i - 1][n]); PIN(bh[i - 1][n]); acca[n] = mfma32(cat8(bl[i - 1][n], bh[i - 1][n]), __builtin_bit_cast(bf16x8, oq[i - 1]), acca[n]); }
        SCHED();
      }
    }
    WAIT_L(0);
#pragma unroll
    for (int n = 0; n < 4; ++n) { PIN(bl[7][n]); PIN(bh[7][n]); acca[n] = mfma32(cat8(bl[7][n], bh[7][n]), __builtin_bit_cast(bf16x8, oq[7]), acca[n]); }
    if (blockIdx.x == 0 && wid == 7) {
#pragma unroll 1
      for (int k = 0; k < 4; ++k) poll_flags(ws, (unsigned)(WS_FO + (k * 64 + lane) * 8), tag);
      if (lane == 0) __hip_atomic_store((GAS u64*)(ws + WS_EP), tag, RLX_AGENT);
    }
  }
  {
    WAIT_V(0); BAR();
    bf16x4 wl[11][4], wh[11][4];
#pragma unroll
    for (int i = 0; i < 11; ++i) {
      const unsigned so = (8 + i) * SLOT;
      tr_frag<0>(wl[i][0], wh[i][0], w_rd_e + so); tr_frag<0>(wl[i][1], wh[i][1], w_rd_o + so);
      tr_frag<512>(wl[i][2], wh[i][2], w_rd_e + so); tr_frag<512>(wl[i][3], wh[i][3], w_rd_o + so);
      if ((i & 1) || i == 10) WAIT_L(0);
    }
    if (wid == 0) poll_flags(ws, (unsigned)(WS_FU + (tt * 8 + (lane & 7)) * 8), tag);
    BAR();
    u32x4 aq[11];
#pragma unroll
    for (int i = 0; i < 11; ++i) aq[i] = ld16_sc1(ruf, lane * 16, (t16a * NUK + i * 2 + kp) * 1024);
    f32x4 accb[4];
#pragma unroll
    for (int n = 0; n < 4; ++n) accb[n] = (f32x4){0.f, 0.f, 0.f, 0.f};
#pragma unroll
    for (int i = 0; i < 11; ++i) {
#pragma unroll
      for (int n = 0; n < 4; ++n) { PIN(wl[i][n]); PIN(wh[i][n]); accb[n] = mfma32(cat8(wl[i][n], wh[i][n]), __builtin_bit_cast(bf16x8, aq[i]), accb[n]); }
    }
#pragma unroll
    for (int n = 0; n < 4; ++n)
      *(LAS f32x4*)(smem + kp * 17408 + (mw * 16 + fr) * 272 + (n * 16 + fq * 4) * 4) = acca[n] * gate + accb[n] * (1.0f - gate);
    WAIT_L(0); BAR();
    const int r = tid >> 3, ch = tid & 7;
    lds_char* p = smem + r * 272 + ch * 32;
    f32x4 a0 = *(LAS f32x4*)p + *(LAS f32x4*)(p + 17408);
    f32x4 a1 = *(LAS f32x4*)(p + 16) + *(LAS f32x4*)(p + 17408 + 16);
    u32x4 w;
    w.x = pk_bf16(a0[0], a0[1]); w.y = pk_bf16(a0[2], a0[3]); w.z = pk_bf16(a1[0], a1[1]); w.w = pk_bf16(a1[2], a1[3]);
    *(u32x4*)((char*)out + (size_t)(tt * 64 + r) * 1024 + nj * 128 + ch * 16) = w;
  }
}

extern "C" void kernel_launch(void* const* d_in, const int* in_sizes, int n_in, void* d_out, int out_size, void* d_ws,
                              size_t ws_size, hipStream_t stream) {
  (void)hipFuncSetAttribute((const void*)gm_fused, hipFuncAttributeMaxDynamicSharedMemorySize, LDS_BYTES);
  gm_fused<<<256, 512, LDS_BYTES, stream>>>((const __hip_bfloat16*)d_in[0], (const __hip_bfloat16*)d_in[1], (const __hip_bfloat16*)d_in[2],
                                            (const __hip_bfloat16*)d_in[4], (const __hip_bfloat16*)d_in[5], (const __hip_bfloat16*)d_in[6],
                                            (const __hip_bfloat16*)d_in[7], (const __hip_bfloat16*)d_in[8], (char*)d_ws, (__hip_bfloat16*)d_out);
}
```
